```python
import torch

def _softmax(x, axis=-1):
    return torch.softmax(x, dim=axis)
def _gelu(x):
    return torch.nn.functional.gelu(x, approximate="tanh")
def _silu(x):
    return torch.nn.functional.silu(x)
def _sigmoid(x):
    return torch.sigmoid(x)
def _layernorm(x, axis=-1, eps=1e-5):
    m = x.mean(dim=axis, keepdim=True)
    v = x.var(dim=axis, keepdim=True, unbiased=False)
    return (x - m) / torch.sqrt(v + eps)
def _scatter_add(tgt, idx, src, axis=0):
    return tgt.clone().index_add_(axis, idx, src)
def _segment_sum(src, seg, n):
    out = torch.zeros((n,) + src.shape[1:], dtype=src.dtype)
    return out.index_add_(0, seg, src)
def _pad(x, pw):
    flat = tuple(v for pair in reversed(pw) for v in pair)
    return torch.nn.functional.pad(x, flat)

kernel_name = 'interleave_layers__linear_attn_block__dense_activation_silu__prompt'

def setup_inputs():

    _w = (lambda a: (lambda t: t.to(torch.bfloat16) if t.is_floating_point() else t)(torch.as_tensor(a)))
    def _A_setup():
        B, S, D = 1, 2048, 512
        return {
            "x": _w((lambda *s: torch.randn(*s, dtype=torch.bfloat16) * 0.05)(B, S, D)),
            "wqkv": _w((lambda *s: torch.randn(*s, dtype=torch.bfloat16) * 0.05)(D, 3 * D)),
            "wo": _w((lambda *s: torch.randn(*s, dtype=torch.bfloat16) * 0.05)(D, D)),
        }
    _A_in = _A_setup()

    def _B_setup():
        B, S, D, F = 1, 2048, 512, 1792
        return {
            "x": _w((lambda *s: torch.randn(*s, dtype=torch.bfloat16) * 0.05)(B, S, D)),
            "w1": _w((lambda *s: torch.randn(*s, dtype=torch.bfloat16) * 0.8)(D, F) * (D ** -0.5)),
            "w2": _w((lambda *s: torch.randn(*s, dtype=torch.bfloat16) * 0.8)(F, D) * (F ** -0.5)),
        }
    _B_in = _B_setup()

    return {
        "A_x": _A_in["x"],
        "A_wqkv": _A_in["wqkv"],
        "A_wo": _A_in["wo"],
        "B_x": _B_in["x"],
        "B_w1": _B_in["w1"],
        "B_w2": _B_in["w2"],
    }

def reference(A_x, A_wqkv, A_wo, B_x, B_w1, B_w2):
    def _A_ref(x, wqkv, wo):
        B, S, D = x.shape
        H = 32; Dh = D // H
        qkv = (lambda x, *d: x.permute(*d))((x @ wqkv).reshape(B, S, 3, H, Dh), 2, 0, 3, 1, 4)
        q, k, v = qkv[0], qkv[1], qkv[2]
        phi = lambda t: torch.where(t > 0, t + 1, torch.exp(t)) + 1e-6
        pq, pk = phi(q), phi(k)
        kv = torch.einsum("bhsd,bhse->bhde", pk, v)
        z = torch.einsum("bhsd->bhd", pk)
        num = torch.einsum("bhqd,bhde->bhqe", pq, kv)
        den = torch.einsum("bhqd,bhd->bhq", pq, z)[..., None] + 1e-6
        o = num / den
        return (lambda x, *d: x.permute(*d))(o, 0, 2, 1, 3).reshape(B, S, D) @ wo

    def _B_ref(x, w1, w2):
        h = torch.einsum("bsd,df->bsf", x, w1)
        h = _silu(h)
        out = torch.einsum("bsf,fd->bsd", h, w2)
        return out

    x = A_x
    x = x + _A_ref(_layernorm(x, axis=-1), A_wqkv, A_wo)
    x = x + _B_ref(_layernorm(x, axis=-1), B_w1, B_w2)
    x = x + _A_ref(_layernorm(x, axis=-1), A_wqkv, A_wo)
    x = x + _B_ref(_layernorm(x, axis=-1), B_w1, B_w2)
    return x
```

```cpp
#include <hip/hip_runtime.h>
#include <stdint.h>
#include <utility>

namespace {

typedef unsigned v4u __attribute__((ext_vector_type(4)));
typedef unsigned v2u __attribute__((ext_vector_type(2)));
typedef float f32x4 __attribute__((ext_vector_type(4)));
typedef float f32x2 __attribute__((ext_vector_type(2)));
typedef __bf16 bf2 __attribute__((ext_vector_type(2)));
typedef __bf16 bf8 __attribute__((ext_vector_type(8)));
typedef short s4v __attribute__((ext_vector_type(4)));
typedef unsigned short u16;
#define DI __device__ __forceinline__
#define GAS __attribute__((address_space(1)))
#define LAS __attribute__((address_space(3)))
typedef GAS unsigned gu32;
#define RLX_AGENT __ATOMIC_RELAXED, __HIP_MEMORY_SCOPE_AGENT
typedef __amdgpu_buffer_rsrc_t rsrc_t;

constexpr unsigned OFF_FLAGS = 0;
constexpr unsigned OFF_WQKV = 65536;
constexpr unsigned OFF_WO = OFF_WQKV + 1536u * 1024u;
constexpr unsigned OFF_W1 = OFF_WO + 512u * 1024u;
constexpr unsigned OFF_W2 = OFF_W1 + 1792u * 1024u;
constexpr unsigned OFF_CS1 = OFF_W2 + 1792u * 1024u;
constexpr unsigned OFF_CSQ = OFF_CS1 + 8u * 1792u * 4u;
constexpr unsigned OFF_PQ = OFF_CSQ + 16u * 96u * 4u;
constexpr unsigned KVREC = 2304;
constexpr unsigned OFF_KVP = OFF_PQ + 256u * 8192u;
constexpr unsigned OFF_ST = OFF_KVP + 2u * 256u * KVREC;
constexpr unsigned ST_BYTES = 128u * 1024u;
constexpr unsigned OFF_O = OFF_ST + 3u * ST_BYTES;
constexpr unsigned XF_BYTES = 128u * 16u * 1024u;
constexpr unsigned OFF_X1 = OFF_O + 2u * XF_BYTES;
constexpr unsigned OFF_X2 = OFF_X1 + 2u * XF_BYTES;
constexpr unsigned HF_BYTES = 128u * 56u * 1024u;
constexpr unsigned OFF_H = OFF_X2 + XF_BYTES;
constexpr unsigned WS_BYTES = OFF_H + 2u * HF_BYTES;
static_assert(OFF_O % 256 == 0 && OFF_KVP % 256 == 0 && OFF_PQ % 256 == 0 && OFF_ST % 256 == 0, "align");

constexpr int FL_TMO = 0;
constexpr int FL_KV = 1;
constexpr int FL_O = 17;
constexpr int FL_X1 = 49;
constexpr int FL_H = 113;
constexpr int FL_X2 = 145;
constexpr unsigned SPIN_CAP = 1u << 17;

constexpr int AUX_SC1 = 16;

constexpr unsigned LU = 16384;
constexpr unsigned L_SCR = 9 * LU;
constexpr unsigned L_CS1 = L_SCR;
constexpr unsigned L_CSQ = L_SCR + 512;
constexpr unsigned L_KVX = L_SCR + 1024;
constexpr unsigned L_STX = L_SCR + 10240;
constexpr unsigned L_STAMP = L_SCR + 12288;
constexpr unsigned L_RED = 6 * LU;
constexpr unsigned K2_LDS = 10 * LU;

DI f32x4 mfma(v4u a, v4u b, f32x4 c) {
  return __builtin_amdgcn_mfma_f32_16x16x32_bf16(__builtin_bit_cast(bf8, a), __builtin_bit_cast(bf8, b), c, 0, 0, 0);
}
DI unsigned pack2(float a, float b) {
  f32x2 v = {a, b};
  return __builtin_bit_cast(unsigned, __builtin_convertvector(v, bf2));
}
DI float bflo(unsigned u) { return __builtin_bit_cast(float, u << 16); }
DI float bfhi(unsigned u) { return __builtin_bit_cast(float, u & 0xffff0000u); }
DI float dot2(unsigned a, unsigned b, float c) {
  return __builtin_amdgcn_fdot2_f32_bf16(__builtin_bit_cast(bf2, a), __builtin_bit_cast(bf2, b), c, false);
}
constexpr float LOG2E = 1.4426950408889634f;
DI float phi(float t) { return __builtin_amdgcn_exp2f(fminf(t, 0.f) * LOG2E) + (fmaxf(t, 0.f) + 1e-6f); }
DI float silu(float t) { return t * __builtin_amdgcn_rcpf(1.0f + __builtin_amdgcn_exp2f(t * -LOG2E)); }
DI v4u ldb(rsrc_t rs, unsigned off) { return __builtin_amdgcn_raw_buffer_load_b128(rs, off, 0, 0); }
DI v4u ldb1(rsrc_t rs, unsigned off) { return __builtin_amdgcn_raw_buffer_load_b128(rs, off, 0, AUX_SC1); }
DI void stb1(rsrc_t rs, unsigned off, v4u v) { __builtin_amdgcn_raw_buffer_store_b128(v, rs, off, 0, AUX_SC1); }
DI void stb1_64(rsrc_t rs, unsigned off, v2u v) { __builtin_amdgcn_raw_buffer_store_b64(v, rs, off, 0, AUX_SC1); }
#define DRAIN() asm volatile("s_waitcnt vmcnt(0)" ::: "memory")
#define LGKM0() asm volatile("s_waitcnt lgkmcnt(0)" ::: "memory")
#define BAR() do { asm volatile("s_waitcnt lgkmcnt(0)" ::: "memory"); __builtin_amdgcn_s_barrier(); asm volatile("" ::: "memory"); } while (0)
#define SCHED() __builtin_amdgcn_sched_barrier(0)
#define PIN(x) asm volatile("" : "+v"(x))
#define PIN4(a, o) asm volatile("" : "+v"(a[(o)]), "+v"(a[(o) + 1]), "+v"(a[(o) + 2]), "+v"(a[(o) + 3]))
#define STAMP(i) do {} while (0)
#define STAMP_INIT() do {} while (0)
#define STAMP_FLUSH(lo, hi) do {} while (0)

template <int... I, typename F> DI void static_for_impl(std::integer_sequence<int, I...>, F&& f) { (f(std::integral_constant<int, I>{}), ...); }
template <int N, typename F> DI void static_for(F&& f) { static_for_impl(std::make_integer_sequence<int, N>{}, f); }

DI void dma16(const void* sbase, unsigned voff, unsigned lds_dst) {
  unsigned keep;
  asm volatile("s_mov_b32 %0, m0\n\ts_mov_b32 m0, %3\n\ts_nop 0\n\tglobal_load_lds_dwordx4 %1, %2\n\ts_mov_b32 m0, %0"
               : "=&s"(keep) : "v"(voff), "s"(sbase), "s"(lds_dst) : "memory");
}
DI v4u tr_frag(LAS unsigned char* p) {
  s4v lo = __builtin_amdgcn_ds_read_tr16_b64_v4i16((LAS s4v*)(p));
  s4v hi = __builtin_amdgcn_ds_read_tr16_b64_v4i16((LAS s4v*)(p + 256));
  v2u l2 = __builtin_bit_cast(v2u, lo), h2 = __builtin_bit_cast(v2u, hi);
  return v4u{l2.x, l2.y, h2.x, h2.y};
}
DI float sum16(float v) {
  const unsigned u = __builtin_bit_cast(unsigned, v);
  unsigned u2 = u; PIN(u2);
  auto r = __builtin_amdgcn_permlane16_swap(u, u2, false, false);
  const unsigned a = r[0], b = r[1];
  return __builtin_bit_cast(float, a) + __builtin_bit_cast(float, b);
}
DI float sum32(float v) {
  const unsigned u = __builtin_bit_cast(unsigned, v);
  unsigned u2 = u; PIN(u2);
  auto r = __builtin_amdgcn_permlane32_swap(u, u2, false, false);
  const unsigned a = r[0], b = r[1];
  return __builtin_bit_cast(float, a) + __builtin_bit_cast(float, b);
}
DI float sum_q(float v) { return sum32(sum16(v)); }
DI v4u pair16(unsigned a0, unsigned a1, unsigned b0, unsigned b1) {
  auto r0 = __builtin_amdgcn_permlane16_swap(a0, b0, false, false);
  auto r1 = __builtin_amdgcn_permlane16_swap(a1, b1, false, false);
  const unsigned x0 = r0[0], x1 = r1[0], x2 = r0[1], x3 = r1[1];
  return v4u{x0, x1, x2, x3};
}

DI void poll_ge(gu32* flags, int idx, unsigned want) {
  gu32* wd = flags + idx * 64;
  for (unsigned sp = 0;;) {
    if (__hip_atomic_load(wd, RLX_AGENT) >= want) return;
    if (++sp > SPIN_CAP) { __hip_atomic_store(flags + FL_TMO * 64, 1u, RLX_AGENT); return; }
    if ((sp & 255u) == 0u && __hip_atomic_load(flags + FL_TMO * 64, RLX_AGENT) != 0u) return;
  }
}

struct Stats { float a[2], b[2]; };
DI void finish_stats(float s1, float s2, float& a, float& b) {
  const float mean = s1 * (1.0f / 512.0f);
  const float var = fmaxf(s2 * (1.0f / 512.0f) - mean * mean, 0.f);
  a = __builtin_amdgcn_rsqf(var + 1e-5f);
  b = -mean * a;
}

DI void kv_epilogue(const f32x4 (&acc)[4][2], LAS unsigned char* stx, LAS float* csq, int lane, f32x4& kva, f32x4& kvb, f32x4& za, f32x4& zb) {
  const int q = lane >> 4, n = lane & 15;
  f32x4 ab[2][2];
#pragma unroll
  for (int rt = 0; rt < 2; ++rt) { ab[rt][0] = *(LAS f32x4*)(stx + rt * 128 + q * 32); ab[rt][1] = *(LAS f32x4*)(stx + rt * 128 + q * 32 + 16); }
  v4u PK[2], V[2];
#pragma unroll
  for (int h = 0; h < 2; ++h) {
    const float ck = csq[(2 * h) * 16 + n], cv = csq[(2 * h + 1) * 16 + n];
    unsigned pk[4], pv[4];
#pragma unroll
    for (int rt = 0; rt < 2; ++rt) {
      float a[4], b[4];
#pragma unroll
      for (int r = 0; r < 4; ++r) {
        const float sa = ab[rt][r >> 1][(r & 1) * 2], sb = ab[rt][r >> 1][(r & 1) * 2 + 1];
        a[r] = phi(fmaf(acc[2 * h][rt][r], sa, sb * ck));
        b[r] = fmaf(acc[2 * h + 1][rt][r], sa, sb * cv);
      }
      pk[2 * rt] = pack2(a[0], a[1]); pk[2 * rt + 1] = pack2(a[2], a[3]);
      pv[2 * rt] = pack2(b[0], b[1]); pv[2 * rt + 1] = pack2(b[2], b[3]);
    }
    PK[h] = v4u{pk[0], pk[1], pk[2], pk[3]};
    V[h] = v4u{pv[0], pv[1], pv[2], pv[3]};
  }
  const f32x4 z4 = {0, 0, 0, 0};
  const v4u ones = {0x3f803f80u, 0x3f803f80u, 0x3f803f80u, 0x3f803f80u};
  kva = mfma(PK[0], V[0], z4); kvb = mfma(PK[1], V[1], z4);
  za = mfma(PK[0], ones, z4); zb = mfma(PK[1], ones, z4);
}
struct KvEpi {
  f32x4 ab[2][2];
  float c[4];
  float y[16], m[16];
  unsigned pk[2][4], pv[2][4];
  DI void load(LAS unsigned char* stx, LAS float* csq, int lane) {
    const int q = lane >> 4, n = lane & 15;
#pragma unroll
    for (int rt = 0; rt < 2; ++rt) { ab[rt][0] = *(LAS f32x4*)(stx + rt * 128 + q * 32); ab[rt][1] = *(LAS f32x4*)(stx + rt * 128 + q * 32 + 16); }
#pragma unroll
    for (int t = 0; t < 4; ++t) c[t] = csq[t * 16 + n];
  }
  DI float sa(int rt, int r) const { return ab[rt][r >> 1][(r & 1) * 2]; }
  DI float sb(int rt, int r) const { return ab[rt][r >> 1][(r & 1) * 2 + 1]; }
  template <int S> DI void step(const f32x4 (&acc)[4][2]) {
    constexpr int st = S / 4, h = (S % 4) >> 1, rt = S & 1, o = (S % 4) * 4;
#pragma unroll
    for (int r = 0; r < 4; ++r) {
      if constexpr (st == 0) { y[o + r] = sb(rt, r) * c[2 * h]; m[o + r] = sb(rt, r) * c[2 * h + 1]; }
      if constexpr (st == 1) { y[o + r] = fmaf(acc[2 * h][rt][r], sa(rt, r), y[o + r]); m[o + r] = fmaf(acc[2 * h + 1][rt][r], sa(rt, r), m[o + r]); }
      if constexpr (st == 3) m[o + r] = fminf(y[o + r], 0.f) * LOG2E;
      if constexpr (st == 4) m[o + r] = __builtin_amdgcn_exp2f(m[o + r]);
      if constexpr (st == 5) y[o + r] = fmaxf(y[o + r], 0.f) + 1e-6f;
      if constexpr (st == 6) y[o + r] = y[o + r] + m[o + r];
    }
    if constexpr (st == 0 || st == 1 || st == 5 || st == 6) PIN4(y, o);
    if constexpr (st == 0 || st == 1 || st == 3 || st == 4) PIN4(m, o);
    if constexpr (st == 2) { pv[h][2 * rt] = pack2(m[o], m[o + 1]); pv[h][2 * rt + 1] = pack2(m[o + 2], m[o + 3]); PIN(pv[h][2 * rt]); PIN(pv[h][2 * rt + 1]); }
    if constexpr (st == 7) { pk[h][2 * rt] = pack2(y[o], y[o + 1]); pk[h][2 * rt + 1] = pack2(y[o + 2], y[o + 3]); PIN(pk[h][2 * rt]); PIN(pk[h][2 * rt + 1]); }
  }
  DI void finish(f32x4& kva, f32x4& kvb, f32x4& za, f32x4& zb) {
    const f32x4 z4 = {0, 0, 0, 0};
    const v4u ones = {0x3f803f80u, 0x3f803f80u, 0x3f803f80u, 0x3f803f80u};
    const v4u PK0 = {pk[0][0], pk[0][1], pk[0][2], pk[0][3]}, PK1 = {pk[1][0], pk[1][1], pk[1][2], pk[1][3]};
    const v4u V0 = {pv[0][0], pv[0][1], pv[0][2], pv[0][3]}, V1 = {pv[1][0], pv[1][1], pv[1][2], pv[1][3]};
    kva = mfma(PK0, V0, z4); kvb = mfma(PK1, V1, z4);
    za = mfma(PK0, ones, z4); zb = mfma(PK1, ones, z4);
  }
};
DI void q_epilogue(const f32x4 (&accq)[2][2], const Stats& st, LAS float* csq, int lane, v4u (&pq)[2]) {
  const int q = lane >> 4;
#pragma unroll
  for (int rt = 0; rt < 2; ++rt) {
    unsigned p[4];
#pragma unroll
    for (int t = 0; t < 2; ++t) {
      const f32x4 c = *(LAS f32x4*)(csq + (4 + t) * 16 + 4 * q);
      float v[4];
#pragma unroll
      for (int r = 0; r < 4; ++r) v[r] = phi(fmaf(accq[t][rt][r], st.a[rt], st.b[rt] * c[r]));
      p[2 * t] = pack2(v[0], v[1]); p[2 * t + 1] = pack2(v[2], v[3]);
    }
    pq[rt] = v4u{p[0], p[1], p[2], p[3]};
  }
}

template <bool SC1>
DI void kv_publish(LAS unsigned char* L, unsigned kvx, rsrc_t rs, unsigned rec_off, int lane, int w, f32x4 kva, f32x4 kvb, f32x4 za, f32x4 zb) {
  LAS unsigned char* me = L + kvx + w * 2176;
  *(LAS f32x4*)(me + lane * 16) = kva;
  *(LAS f32x4*)(me + 1024 + lane * 16) = kvb;
  if ((lane & 15) == 0) {
    *(LAS f32x4*)(me + 2048 + (lane >> 4) * 16) = za;
    *(LAS f32x4*)(me + 2112 + (lane >> 4) * 16) = zb;
  }
  BAR();
  if (w < 2 || (w == 2 && lane < 8)) {
    const unsigned o = w * 1024 + lane * 16;
    f32x4 s = *(LAS f32x4*)(L + kvx + o);
#pragma unroll
    for (int ww = 1; ww < 4; ++ww) s += *(LAS f32x4*)(L + kvx + ww * 2176 + o);
    if (SC1) stb1(rs, rec_off + o, __builtin_bit_cast(v4u, s));
    else __builtin_amdgcn_raw_buffer_store_b128(__builtin_bit_cast(v4u, s), rs, rec_off + o, 0, 0);
  }
}

constexpr unsigned K1_W = 0;
constexpr unsigned K1_XB = 98304;
constexpr unsigned K1_CS = K1_XB + 32768;
constexpr unsigned K1_CSF = K1_CS + 1536;
constexpr unsigned K1_KVX = K1_CSF + 512;
constexpr unsigned K1_STX = K1_KVX + 4 * 2176;
constexpr unsigned K1_STAMP = K1_STX + 1024;
constexpr unsigned K1_LDS = K1_STAMP + 512;

__global__ void __launch_bounds__(256, 1) k1(const u16* __restrict__ x, const u16* __restrict__ wqkv, const u16* __restrict__ wo,
                                             const u16* __restrict__ w1, const u16* __restrict__ w2, unsigned char* __restrict__ ws) {
  extern __shared__ __attribute__((aligned(16))) unsigned char smem[];
  LAS unsigned char* L = (LAS unsigned char*)smem;
  constexpr unsigned LSTAMP = K1_STAMP;
  const unsigned lbase = (unsigned)(size_t)L;
  const int tid = threadIdx.x, lane = tid & 63, w = __builtin_amdgcn_readfirstlane(tid >> 6);
  const int b = blockIdx.x, g = 4 * ((b & 7) >> 1) + (b >> 6), j = 8 * (b & 1) + ((b >> 3) & 7);
  const rsrc_t rs = __builtin_amdgcn_make_buffer_rsrc((void*)ws, 0, WS_BYTES, 0x00020000);
  const int q = lane >> 4, n = lane & 15;
  STAMP_INIT();
  STAMP(0);

  const int drow = lane >> 2, dch = (lane & 3) ^ (((drow >> 3) & 1) << 1);
  {
    const unsigned voff = (unsigned)(drow * 1536 + 32 * j + dch * 8) * 2u;
#pragma unroll
    for (int i = 0; i < 24; ++i) {
      const int it = 4 * i + w, rb = it / 3, seg = it % 3;
      dma16((const unsigned char*)wqkv + (size_t)(rb * 16 * 1536 + seg * 512) * 2, voff, lbase + K1_W + (seg * 32 + rb) * 1024);
    }
  }
  v4u xr[4][8];
  {
    const GAS v4u* xp = (const GAS v4u*)(x + (size_t)(128 * g + 32 * w + (lane >> 5)) * 512 + (lane & 31) * 8);
#pragma unroll
    for (int blk = 0; blk < 4; ++blk)
#pragma unroll
      for (int i = 0; i < 8; ++i) xr[blk][i] = xp[((blk >> 1) * 16 + 2 * i) * 64 + (blk & 1) * 32];
  }
  const u16* psrc; int pld, pp, pks, pkind;
  if (b < 112) { psrc = w1; pld = 1792; pp = b >> 1; pks = 8 * (b & 1) + 2 * w; pkind = 0; }
  else {
    const int u = (b - 112) * 8 + 2 * w;
    if (u < 896) { psrc = w2; pld = 512; pp = u / 56; pks = u % 56; pkind = 1; }
    else { psrc = wo; pld = 512; pp = (u - 896) >> 4; pks = (u - 896) & 15; pkind = 2; }
  }
  v4u pt[4];
  {
    const GAS unsigned char* pb = (const GAS unsigned char*)(psrc + (size_t)(pks * 32 + drow) * pld + 32 * pp + dch * 8);
#pragma unroll
    for (int i = 0; i < 4; ++i) pt[i] = *(const GAS v4u*)(pb + (size_t)i * 16 * pld * 2);
  }
  SCHED();
  if (tid < 16) stb1(rs, OFF_FLAGS + b * 256 + tid * 16, v4u{0u, 0u, 0u, 0u});
  STAMP(1);

  v4u xa[16], xb[16];
  float s1a = 0, s2a = 0, s1b = 0, s2b = 0;
  {
    LAS unsigned char* B = L + K1_XB + w * 8192;
    const int c = lane & 31, ksl = c >> 2, kgw = c & 3, fw = (ksl & 1) * 4 + kgw;
    LAS unsigned char* wp = B + ksl * 1024 + kgw * 256;
    LAS unsigned char* rp0 = B + q * 256 + ((n ^ q) * 16);
    LAS unsigned char* rp1 = B + q * 256 + ((n ^ (4 + q)) * 16);
#pragma unroll
    for (int blk = 0; blk < 4; ++blk) {
#pragma unroll
      for (int i = 0; i < 8; ++i) *(LAS v4u*)(wp + (((2 * i + (lane >> 5)) ^ fw) * 16)) = xr[blk][i];
      LGKM0();
#pragma unroll
      for (int k8 = 0; k8 < 8; ++k8) {
        const v4u f = *(LAS v4u*)(((k8 & 1) ? rp1 : rp0) + k8 * 1024);
        if (blk < 2) xa[(blk & 1) * 8 + k8] = f; else xb[(blk & 1) * 8 + k8] = f;
#pragma unroll
        for (int e = 0; e < 4; ++e) {
          if (blk < 2) { s1a = dot2(f[e], 0x3f803f80u, s1a); s2a = dot2(f[e], f[e], s2a); }
          else { s1b = dot2(f[e], 0x3f803f80u, s1b); s2b = dot2(f[e], f[e], s2b); }
        }
      }
      PIN(s1a); PIN(s2a); PIN(s1b); PIN(s2b);
      LGKM0();
    }
  }
  STAMP(2);
  const int r4 = n >> 2, c4 = n & 3;
  unsigned trb[2];
#pragma unroll
  for (int ct = 0; ct < 2; ++ct)
    trb[ct] = (unsigned)((q >> 1) * 1024 + ((q & 1) * 8 + r4) * 64 + (2 * (ct ^ (q & 1)) + (c4 >> 1)) * 16 + (c4 & 1) * 8);

  const f32x4 z4 = {0, 0, 0, 0};
  const v4u ones = {0x3f803f80u, 0x3f803f80u, 0x3f803f80u, 0x3f803f80u};
  {
    LAS unsigned char* B = L + K1_XB + w * 8192;
#pragma unroll
    for (int i = 0; i < 4; ++i) *(LAS v4u*)(B + i * 1024 + lane * 16) = pt[i];
    LGKM0();
    f32x4 c1[2] = {z4, z4};
#pragma unroll
    for (int u = 0; u < 2; ++u)
#pragma unroll
      for (int ct = 0; ct < 2; ++ct) {
        const v4u f = tr_frag(B + u * 2048 + trb[ct]);
        const int gct = 2 * pp + ct, ks = pks + u;
        unsigned dst;
        if (pkind == 0) dst = OFF_W1 + (unsigned)((gct / 7) * 112 + (gct % 7) * 16 + ks) * 1024u;
        else if (pkind == 1) dst = OFF_W2 + (unsigned)(((gct >> 2) * 4 + ks / 14) * 56 + (ks % 14) * 4 + (gct & 3)) * 1024u;
        else dst = OFF_WO + (unsigned)((gct >> 2) * 64 + (gct & 3) * 16 + ks) * 1024u;
        stb1(rs, dst + lane * 16, f);
        c1[ct] = mfma(f, ones, c1[ct]);
      }
    if (pkind == 0 && n == 0) {
      const int part = (b & 1) * 4 + w;
#pragma unroll
      for (int ct = 0; ct < 2; ++ct)
        stb1(rs, OFF_CS1 + (unsigned)(part * 1792 + 32 * pp + 16 * ct + 4 * q) * 4u, __builtin_bit_cast(v4u, c1[ct]));
    }
  }
  Stats st;
  finish_stats(sum_q(s1a), sum_q(s2a), st.a[0], st.b[0]);
  finish_stats(sum_q(s1b), sum_q(s2b), st.a[1], st.b[1]);
  if (q == 0) {
    *(LAS f32x2*)(L + K1_STX + w * 256 + n * 8) = f32x2{st.a[0], st.b[0]};
    *(LAS f32x2*)(L + K1_STX + w * 256 + 128 + n * 8) = f32x2{st.a[1], st.b[1]};
  }
  STAMP(3);
  DRAIN();
  BAR();
  STAMP(4);

  auto wimg = [&](int t, int ks) -> LAS unsigned char* {
    const int seg = t < 4 ? 1 + (t & 1) : 0, ct = t < 4 ? (t >> 1) : (t - 4);
    return L + K1_W + seg * 32768 + ks * 2048 + trb[ct];
  };
  {
    f32x4 cs[6] = {z4, z4, z4, z4, z4, z4};
#pragma unroll
    for (int i = 0; i < 4; ++i)
#pragma unroll
      for (int t = 0; t < 6; ++t) {
        const v4u f = tr_frag(wimg(t, 0) + (4 * w + i) * 2048);
        cs[t] = (t >= 4) ? mfma(f, ones, cs[t]) : mfma(ones, f, cs[t]);
      }
    LAS float* csw = (LAS float*)(L + K1_CS) + w * 96;
#pragma unroll
    for (int t = 4; t < 6; ++t) if (n == 0) *(LAS f32x4*)(csw + t * 16 + 4 * q) = cs[t];
#pragma unroll
    for (int t = 0; t < 4; ++t) if (q == 0) csw[t * 16 + n] = cs[t][0];
    stb1(rs, OFF_WQKV + (unsigned)(j * 96 + w * 16 + g) * 1024u + lane * 16, tr_frag(L + K1_W + (1 + (w & 1)) * 32768 + g * 2048 + trb[w >> 1]));
    if (w < 2) stb1(rs, OFF_WQKV + (unsigned)(j * 96 + (4 + w) * 16 + g) * 1024u + lane * 16, tr_frag(L + K1_W + g * 2048 + trb[w]));
  }
  f32x4 acc[4][2], accq[2][2];
#pragma unroll
  for (int t = 0; t < 4; ++t) { acc[t][0] = z4; acc[t][1] = z4; }
#pragma unroll
  for (int t = 0; t < 2; ++t) { accq[t][0] = z4; accq[t][1] = z4; }
  {
    constexpr int FD = 6, NF = 96;
    auto addr = [&](int i) -> LAS unsigned char* { return i < 64 ? wimg(i & 3, i >> 2) : wimg(4 + ((i - 64) & 1), (i - 64) >> 1); };
    v4u wf[FD];
#pragma unroll
    for (int i = 0; i < FD; ++i) wf[i] = tr_frag(addr(i));
    SCHED();
    static_for<NF>([&](auto I) {
      constexpr int i = I;
      const v4u f = wf[i % FD];
      if constexpr (i + FD < NF) wf[i % FD] = tr_frag(addr(i + FD));
      if constexpr (i < 64) {
        constexpr int t = i & 3, ks = i >> 2;
        acc[t][0] = mfma(xa[ks], f, acc[t][0]); acc[t][1] = mfma(xb[ks], f, acc[t][1]);
      } else {
        constexpr int t = (i - 64) & 1, ks = (i - 64) >> 1;
        accq[t][0] = mfma(f, xa[ks], accq[t][0]); accq[t][1] = mfma(f, xb[ks], accq[t][1]);
      }
      SCHED();
    });
  }
  STAMP(5);
  BAR();
  if (tid < 96) {
    LAS float* c = (LAS float*)(L + K1_CS);
    const float s = (c[tid] + c[96 + tid]) + (c[192 + tid] + c[288 + tid]);
    ((LAS float*)(L + K1_CSF))[tid] = s;
    if (g == 0) ((float*)(ws + OFF_CSQ))[j * 96 + tid] = s;
  }
  BAR();
  v4u pq[2]; f32x4 kva, kvb, za, zb;
  kv_epilogue(acc, L + K1_STX + w * 256, (LAS float*)(L + K1_CSF), lane, kva, kvb, za, zb);
  q_epilogue(accq, st, (LAS float*)(L + K1_CSF), lane, pq);
  const int cu = g * 16 + j;
#pragma unroll
  for (int rt = 0; rt < 2; ++rt) stb1(rs, OFF_PQ + (unsigned)((cu * 4 + w) * 2 + rt) * 1024u + lane * 16, pq[rt]);
  kv_publish<true>(L, K1_KVX, rs, OFF_KVP + (unsigned)(j * 16 + g) * KVREC, lane, w, kva, kvb, za, zb);
  STAMP(6);
  STAMP_FLUSH(0, 8);
}

struct LBase { LAS unsigned char* b[3]; };
DI v4u ldsf(const LBase& lb, unsigned off) { return *(LAS v4u*)(lb.b[off >> 16] + (off & 0xffffu)); }

DI void publish(gu32* flags, int idx, int tid) {
  DRAIN();
  BAR();
  if (tid == 0) __hip_atomic_fetch_add(flags + idx * 64, 1u, RLX_AGENT);
}
template <int NKB>
DI void dma_block(const unsigned char* ws, unsigned src, unsigned ldst, int w, int lane) {
  static_assert(NKB % 16 == 0, "");
  const unsigned char* p = ws + src + (unsigned)w * (NKB / 4) * 1024u;
  unsigned d = ldst + (unsigned)w * (NKB / 4) * 1024u;
#pragma unroll 1
  for (int i = 0; i < NKB / 16; ++i) {
    dma16(p, (unsigned)lane * 16u, d);
    dma16(p + 1024, (unsigned)lane * 16u, d + 1024);
    dma16(p + 2048, (unsigned)lane * 16u, d + 2048);
    dma16(p + 3072, (unsigned)lane * 16u, d + 3072);
    p += 4096; d += 4096;
  }
}
template <int NKB>
DI void dma_w123(const unsigned char* ws, unsigned src, unsigned ldst, int w, int lane) {
  if (w == 0) return;
  const unsigned char* p = ws + src + (unsigned)(w - 1) * 1024u;
  unsigned d = ldst + (unsigned)(w - 1) * 1024u;
#pragma unroll 1
  for (int i = w - 1; i < NKB; i += 3) {
    dma16(p, (unsigned)lane * 16u, d);
    p += 3072; d += 3072;
  }
}
DI void res_update(v2u (&res)[4], const f32x4 (&acc)[4]) {
#pragma unroll
  for (int c = 0; c < 4; ++c) {
    const unsigned t0 = pack2(acc[c][0], acc[c][1]), t1 = pack2(acc[c][2], acc[c][3]);
    res[c].x = pack2(bflo(res[c].x) + bflo(t0), bfhi(res[c].x) + bfhi(t0));
    res[c].y = pack2(bflo(res[c].y) + bflo(t1), bfhi(res[c].y) + bfhi(t1));
  }
}
DI void store_x_tile(rsrc_t rs, unsigned xoff, unsigned stoff, const v2u (&res)[4], int myrt, int cs, int lane) {
  const int q = lane >> 4, n = lane & 15;
  const unsigned kg = (q & 1) * 2 + (q >> 1);
#pragma unroll
  for (int c2 = 0; c2 < 2; ++c2) {
    const v4u o = pair16(res[2 * c2].x, res[2 * c2].y, res[2 * c2 + 1].x, res[2 * c2 + 1].y);
    stb1(rs, xoff + (unsigned)(myrt * 16 + 2 * cs + c2) * 1024u + (kg * 16 + n) * 16, o);
  }
  float s1 = 0, s2 = 0;
#pragma unroll
  for (int c = 0; c < 4; ++c) {
    s1 = dot2(res[c].x, 0x3f803f80u, s1); s2 = dot2(res[c].x, res[c].x, s2);
    s1 = dot2(res[c].y, 0x3f803f80u, s1); s2 = dot2(res[c].y, res[c].y, s2);
  }
  s1 = sum_q(s1); s2 = sum_q(s2);
  if (q == 0) stb1_64(rs, stoff + (unsigned)myrt * 1024u + n * 64 + cs * 8, __builtin_bit_cast(v2u, f32x2{s1, s2}));
}
DI void load_stats(rsrc_t rs, unsigned stoff, int rt0, int lane, v4u (&sp)[2]) {
  const int q = lane >> 4, n = lane & 15;
#pragma unroll
  for (int rt = 0; rt < 2; ++rt) sp[rt] = ldb1(rs, stoff + (unsigned)(rt0 + rt) * 1024u + n * 64 + q * 16);
}
DI void make_stats(const v4u (&sp)[2], Stats& st) {
#pragma unroll
  for (int rt = 0; rt < 2; ++rt) {
    const f32x4 p = __builtin_bit_cast(f32x4, sp[rt]);
    finish_stats(sum_q(p[0] + p[2]), sum_q(p[1] + p[3]), st.a[rt], st.b[rt]);
  }
}

__global__ void __launch_bounds__(256, 1) k2(const u16* __restrict__ x, u16* __restrict__ out, unsigned char* __restrict__ ws) {
  extern __shared__ __attribute__((aligned(16))) unsigned char smem[];
  LAS unsigned char* L = (LAS unsigned char*)smem;
  constexpr unsigned LSTAMP = L_STAMP;
  const unsigned lbase = (unsigned)(size_t)L;
  const int tid = threadIdx.x, lane0 = tid & 63, w = __builtin_amdgcn_readfirstlane(tid >> 6);
  const int b = blockIdx.x, g = 2 * (b & 7) + ((b >> 3) >> 4), j = (b >> 3) & 15;
  const int hh = j >> 3, cs = j & 7;
  const rsrc_t rs = __builtin_amdgcn_make_buffer_rsrc((void*)ws, 0, WS_BYTES, 0x00020000);
  gu32* flags = (gu32*)(ws + OFF_FLAGS);
  const f32x4 z4 = {0, 0, 0, 0};
  const int cu = g * 16 + j;
  const int myrt = 8 * g + 4 * hh + w;
  const int rt0 = 8 * g + 2 * w;
#define FRESH() int lane = lane0; PIN(lane); const int q = lane >> 4, n = lane & 15; const unsigned l16 = (unsigned)lane * 16u; \
  LBase lb; { unsigned o1 = l16 + 65536u, o2 = l16 + 131072u; PIN(o1); PIN(o2); lb.b[0] = L + l16; lb.b[1] = L + o1; lb.b[2] = L + o2; } (void)q; (void)n;
  STAMP_INIT();
  STAMP(8);

  v4u pq[2];
#pragma unroll
  for (int rt = 0; rt < 2; ++rt) pq[rt] = ldb(rs, OFF_PQ + (unsigned)((cu * 4 + w) * 2 + rt) * 1024u + (unsigned)lane0 * 16u);
  v2u res[4];
  float csv = 0.f;

  v4u w2[14][4];

  static_for<2>([&](auto LL) {
    constexpr int l = LL;
    constexpr int SB = 10 + l * 24;
    if (l == 1) {
      if (tid == 0) poll_ge(flags, FL_KV + j, 16u);
      BAR();
    }
    STAMP(SB + 0);
    {
      FRESH();
      const unsigned rb = OFF_KVP + (unsigned)((l * 16 + j) * 16 + 4 * w) * KVREC;
      v4u a[4], bb[4], zz[4];
#pragma unroll
      for (int i = 0; i < 4; ++i) {
        a[i] = ldb1(rs, rb + i * KVREC + l16);
        bb[i] = ldb1(rs, rb + i * KVREC + 1024 + l16);
        zz[i] = ldb1(rs, rb + i * KVREC + 2048 + (lane & 7) * 16);
      }
      if (l == 0) {
        const GAS v2u* xp = (const GAS v2u*)(x + (size_t)(16 * myrt + n) * 512 + 64 * cs + 4 * q);
#pragma unroll
        for (int c = 0; c < 4; ++c) res[c] = xp[c * 4];
        if (tid < 112) {
          const GAS float* p = (const GAS float*)(ws + OFF_CS1) + 112 * j + tid;
#pragma unroll
          for (int i = 0; i < 8; ++i) csv += p[i * 1792];
        } else if (tid >= 128 && tid < 224) {
          csv = ((const GAS float*)(ws + OFF_CSQ))[j * 96 + tid - 128];
        }
      }
      dma_block<64>(ws, OFF_WO + cs * 65536u, lbase, w, lane);
      SCHED();
      f32x4 sa = __builtin_bit_cast(f32x4, a[0]), sb = __builtin_bit_cast(f32x4, bb[0]), sz = __builtin_bit_cast(f32x4, zz[0]);
#pragma unroll
      for (int i = 1; i < 4; ++i) { sa += __builtin_bit_cast(f32x4, a[i]); sb += __builtin_bit_cast(f32x4, bb[i]); sz += __builtin_bit_cast(f32x4, zz[i]); }
      LAS unsigned char* me = L + L_KVX + w * 2176;
      *(LAS f32x4*)(me + l16) = sa;
      *(LAS f32x4*)(me + 1024 + l16) = sb;
      if (lane < 8) *(LAS f32x4*)(me + 2048 + l16) = sz;
      BAR();
      f32x4 kva = z4, kvb = z4, za = z4, zb = z4;
#pragma unroll
      for (int ww = 0; ww < 4; ++ww) {
        LAS unsigned char* p = L + L_KVX + ww * 2176;
        kva += *(LAS f32x4*)(p + l16); kvb += *(LAS f32x4*)(p + 1024 + l16);
        za += *(LAS f32x4*)(p + 2048 + q * 16); zb += *(LAS f32x4*)(p + 2112 + q * 16);
      }
      const v4u Aa = {pack2(kva[0], kva[1]), pack2(kva[2], kva[3]), 0u, 0u};
      const v4u Ab = {0u, 0u, pack2(kvb[0], kvb[1]), pack2(kvb[2], kvb[3])};
      v4u Az = {0u, 0u, 0u, 0u};
      if ((n & 3) == 0) { Az.x = pack2(za[0], za[1]); Az.y = pack2(za[2], za[3]); }
      if ((n & 3) == 1) { Az.z = pack2(zb[0], zb[1]); Az.w = pack2(zb[2], zb[3]); }
#pragma unroll
      for (int rt = 0; rt < 2; ++rt) {
        const f32x4 na = mfma(Aa, pq[rt], z4), nb = mfma(Ab, pq[rt], z4), dn = mfma(Az, pq[rt], z4);
        const float ra = __builtin_amdgcn_rcpf(dn[0] + 1e-6f), rb2 = __builtin_amdgcn_rcpf(dn[1] + 1e-6f);
        const v4u o = pair16(pack2(na[0] * ra, na[1] * ra), pack2(na[2] * ra, na[3] * ra), pack2(nb[0] * rb2, nb[1] * rb2), pack2(nb[2] * rb2, nb[3] * rb2));
        const unsigned kg = (q & 1) * 2 + (q >> 1);
        stb1(rs, OFF_O + l * XF_BYTES + (unsigned)((rt0 + rt) * 16 + j) * 1024u + (kg * 16 + n) * 16, o);
      }
      if (l == 0) {
        if (tid < 112) ((LAS float*)(L + L_CS1))[tid] = csv;
        else if (tid >= 128 && tid < 224) ((LAS float*)(L + L_CSQ))[tid - 128] = csv;
      }
    }
    STAMP(SB + 1);
    publish(flags, FL_O + l * 16 + g, tid);
    STAMP(SB + 2);
    dma_w123<80>(ws, OFF_W1 + j * 114688u, lbase + 4 * LU, w, lane0);

    if (tid == 0) poll_ge(flags, FL_O + l * 16 + g, 16u);
    BAR();
    STAMP(SB + 3);
    {
      FRESH();
      v4u a[16];
#pragma unroll
      for (int ks = 0; ks < 16; ++ks) a[ks] = ldb1(rs, OFF_O + l * XF_BYTES + (unsigned)(myrt * 16 + ks) * 1024u + l16);
      SCHED();
      f32x4 acc[4] = {z4, z4, z4, z4};
      constexpr int FD = 8;
      v4u wf[FD];
#pragma unroll
      for (int i = 0; i < FD; ++i) wf[i] = ldsf(lb, (i & 3) * LU + (i >> 2) * 1024);
      SCHED();
      static_for<64>([&](auto I) {
        constexpr int i = I;
        const v4u f = wf[i % FD];
        if constexpr (i + FD < 64) wf[i % FD] = ldsf(lb, ((i + FD) & 3) * LU + ((i + FD) >> 2) * 1024);
        acc[i & 3] = mfma(f, a[i >> 2], acc[i & 3]);
        if constexpr (i == 0) STAMP(SB + 19);
        if constexpr (i == 63) STAMP(SB + 20);
        SCHED();
      });
      res_update(res, acc);
      store_x_tile(rs, OFF_X1 + l * XF_BYTES, OFF_ST + l * ST_BYTES, res, myrt, cs, lane);
    }
    STAMP(SB + 4);
    publish(flags, FL_X1 + (l * 16 + g) * 2 + hh, tid);
    STAMP(SB + 5);
    dma_w123<32>(ws, OFF_W1 + j * 114688u + 5 * LU, lbase, w, lane0);

    if (tid == 0) { poll_ge(flags, FL_X1 + (l * 16 + g) * 2, 8u); poll_ge(flags, FL_X1 + (l * 16 + g) * 2 + 1, 8u); }
    DRAIN();
    BAR();
    STAMP(SB + 6);
    {
      FRESH();
      const unsigned ab = OFF_X1 + l * XF_BYTES + (unsigned)(rt0 * 16) * 1024u + l16;
      v4u xa[16], xb[16], sp[2];
      constexpr int AD = 6;
      load_stats(rs, OFF_ST + l * ST_BYTES, rt0, lane, sp);
#pragma unroll
      for (int ks = 0; ks < AD; ++ks) { xa[ks] = ldb1(rs, ab + ks * 1024); xb[ks] = ldb1(rs, ab + (16 + ks) * 1024); }
      auto unit = [](int t) { return t < 5 ? 4 + t : t - 5; };
      constexpr int FD = 6, NF = 112;
      auto faddr = [&](int i) -> unsigned { return i < 64 ? unit(i & 3) * LU + (i >> 2) * 1024 : unit(4 + (i - 64) / 16) * LU + ((i - 64) % 16) * 1024; };
      f32x4 acc[7][2];
#pragma unroll
      for (int t = 0; t < 7; ++t) { acc[t][0] = z4; acc[t][1] = z4; }
      v4u wf[FD];
#pragma unroll
      for (int i = 0; i < FD; ++i) wf[i] = ldsf(lb, faddr(i));
      f32x4 csr[2];
      csr[0] = *(LAS f32x4*)(L + L_CS1 + (4 * q) * 4);
      SCHED();
      float sa[2], sb[2];
      float y[8], e[8];
      auto epi = [&](auto T, auto H) {
        constexpr int t = T, st = H / 2, hf = H % 2;
#pragma unroll
        for (int r = 0; r < 4; ++r) {
          const int i = hf * 4 + r;
          if constexpr (st == 0) e[i] = sb[hf] * csr[t & 1][r];
          if constexpr (st == 1) y[i] = fmaf(acc[t][hf][r], sa[hf], e[i]);
          if constexpr (st == 2) e[i] = __builtin_amdgcn_exp2f(-y[i]);
          if constexpr (st == 3) e[i] = fmaf(e[i], LOG2E, LOG2E);
          if constexpr (st == 4) e[i] = __builtin_amdgcn_rcpf(e[i]);
          if constexpr (st == 5) y[i] = y[i] * e[i];
        }
        if constexpr (st == 0 || st == 2 || st == 3 || st == 4) PIN4(e, hf * 4);
        if constexpr (st == 1 || st == 5) PIN4(y, hf * 4);
        if constexpr (st == 1 && hf == 1 && t < 6) csr[(t + 1) & 1] = *(LAS f32x4*)(L + L_CS1 + ((t + 1) * 16 + 4 * q) * 4);
        if constexpr (st == 6 && hf == 0) {
          const v4u o = pair16(pack2(y[0], y[1]), pack2(y[2], y[3]), pack2(y[4], y[5]), pack2(y[6], y[7]));
          const int TT = 7 * j + t;
          stb1(rs, OFF_H + l * HF_BYTES + ((unsigned)(rt0 + (q & 1)) * 56u + (unsigned)(TT >> 1)) * 1024u + ((2 * (TT & 1) + (q >> 1)) * 16 + n) * 16, o);
        }
      };
      static_for<NF>([&](auto I) {
        constexpr int i = I;
        const v4u f = wf[i % FD];
        if constexpr (i + FD < NF) wf[i % FD] = ldsf(lb, faddr(i + FD));
        constexpr int t = i < 64 ? (i & 3) : 4 + (i - 64) / 16, ks = i < 64 ? (i >> 2) : (i - 64) % 16;
        acc[t][0] = mfma(f, xa[ks], acc[t][0]); acc[t][1] = mfma(f, xb[ks], acc[t][1]);
        if constexpr (i < 64 && (i & 3) == 1 && ks + AD < 16) { xa[ks + AD] = ldb1(rs, ab + (ks + AD) * 1024); xb[ks + AD] = ldb1(rs, ab + (16 + ks + AD) * 1024); }
        if constexpr (i >= 44 && i < 58 && l == 0) {
          constexpr int k = i - 44;
          w2[k >> 1][(k & 1) * 2] = ldb(rs, OFF_W2 + (unsigned)(((cs * 4 + w) * 14 + (k >> 1)) * 4 + (k & 1) * 2) * 1024u + l16);
          w2[k >> 1][(k & 1) * 2 + 1] = ldb(rs, OFF_W2 + (unsigned)(((cs * 4 + w) * 14 + (k >> 1)) * 4 + (k & 1) * 2 + 1) * 1024u + l16);
        }
        if constexpr (i == 58) {
          Stats st;
          make_stats(sp, st);
          sa[0] = st.a[0] * LOG2E; sa[1] = st.a[1] * LOG2E; sb[0] = st.b[0] * LOG2E; sb[1] = st.b[1] * LOG2E;
        }
        if constexpr (i == 0) STAMP(SB + 16);
        if constexpr (i == 63) STAMP(SB + 17);
        if constexpr (i == 111) STAMP(SB + 18);
        if constexpr (i == 71) STAMP(SB + 21);
        if constexpr (i == 79) STAMP(SB + 22);
        if constexpr (i == 95) STAMP(SB + 23);
        if constexpr (i >= 64) {
          constexpr int k = i - 64;
          constexpr int h0 = (k * 78) / 48, h1 = ((k + 1) * 78) / 48;
          static_for<h1 - h0>([&](auto D) {
            constexpr int h = h0 + D;
            epi(std::integral_constant<int, h / 13>{}, std::integral_constant<int, h % 13>{});
          });
        }
        SCHED();
      });
      static_for<13>([&](auto H) { epi(std::integral_constant<int, 6>{}, H); });
      if (l == 0) {
#pragma unroll
        for (int i = 7; i < 14; ++i)
#pragma unroll
          for (int c = 0; c < 4; ++c) w2[i][c] = ldb(rs, OFF_W2 + (unsigned)(((cs * 4 + w) * 14 + i) * 4 + c) * 1024u + l16);
        SCHED();
        STAMP(SB + 7);
        asm volatile("s_waitcnt vmcnt(28)" ::: "memory");
        BAR();
        if (tid == 0) __hip_atomic_fetch_add(flags + (FL_H + l * 16 + g) * 64, 1u, RLX_AGENT);
      } else {
        STAMP(SB + 7);
        publish(flags, FL_H + l * 16 + g, tid);
      }
    }
    STAMP(SB + 8);
    if (l == 0) dma_w123<96>(ws, OFF_WQKV + j * 98304u, lbase, w, lane0);

    if (tid == 0) poll_ge(flags, FL_H + l * 16 + g, 16u);
    BAR();
    STAMP(SB + 9);
    {
      FRESH();
      constexpr int WIN = 28;
      v4u a[WIN];
      unsigned hb[4];
#pragma unroll
      for (int i4 = 0; i4 < 4; ++i4) hb[i4] = OFF_H + l * HF_BYTES + (unsigned)((8 * g + 4 * hh + ((w + i4) & 3)) * 56 + 14 * w) * 1024u + l16;
#pragma unroll
      for (int f = 0; f < WIN; ++f) a[f] = ldb1(rs, hb[f & 3] + (f >> 2) * 1024);
      SCHED();
      f32x4 acc[4][4];
#pragma unroll
      for (int i4 = 0; i4 < 4; ++i4)
#pragma unroll
        for (int c = 0; c < 4; ++c) acc[i4][c] = z4;
      static_for<56>([&](auto F) {
        constexpr int f = F;
        const v4u cur = a[f % WIN];
        if constexpr (f + WIN < 56) a[f % WIN] = ldb1(rs, hb[(f + WIN) & 3] + ((f + WIN) >> 2) * 1024);
#pragma unroll
        for (int c = 0; c < 4; ++c) acc[f & 3][c] = mfma(w2[f >> 2][c], cur, acc[f & 3][c]);
        SCHED();
      });
      STAMP(SB + 10);
#pragma unroll
      for (int i4 = 1; i4 < 4; ++i4) {
        const unsigned d = (w + i4) & 3;
#pragma unroll
        for (int c = 0; c < 4; ++c) *(LAS f32x4*)(L + L_RED + ((d * 3 + (i4 - 1)) * 4 + c) * 1024 + l16) = acc[i4][c];
      }
      BAR();
#pragma unroll
      for (int s = 0; s < 3; ++s)
#pragma unroll
        for (int c = 0; c < 4; ++c) acc[0][c] += *(LAS f32x4*)(L + L_RED + ((w * 3 + s) * 4 + c) * 1024 + l16);
      res_update(res, acc[0]);
      if (l == 0) {
        store_x_tile(rs, OFF_X2, OFF_ST + 2 * ST_BYTES, res, myrt, cs, lane);
      } else {
#pragma unroll
        for (int c2 = 0; c2 < 2; ++c2) {
          const v4u o = pair16(res[2 * c2].x, res[2 * c2].y, res[2 * c2 + 1].x, res[2 * c2 + 1].y);
          *(GAS v4u*)(out + (size_t)(16 * myrt + n) * 512 + 64 * cs + 16 * (2 * c2 + (q & 1)) + 8 * (q >> 1)) = o;
        }
      }
    }
    STAMP(SB + 11);
    if (l == 0) {
      publish(flags, FL_X2 + g * 2 + hh, tid);
      STAMP(SB + 12);
      if (tid == 0) { poll_ge(flags, FL_X2 + g * 2, 8u); poll_ge(flags, FL_X2 + g * 2 + 1, 8u); }
      DRAIN();
      BAR();
      STAMP(SB + 13);
      FRESH();
      const unsigned ab = OFF_X2 + (unsigned)(rt0 * 16) * 1024u + l16;
      v4u xa[16], xb[16], sp[2];
      constexpr int AD = 6;
      load_stats(rs, OFF_ST + 2 * ST_BYTES, rt0, lane, sp);
#pragma unroll
      for (int ks = 0; ks < AD; ++ks) { xa[ks] = ldb1(rs, ab + ks * 1024); xb[ks] = ldb1(rs, ab + (16 + ks) * 1024); }
      f32x4 acc[4][2], accq[2][2];
#pragma unroll
      for (int t = 0; t < 4; ++t) { acc[t][0] = z4; acc[t][1] = z4; }
#pragma unroll
      for (int t = 0; t < 2; ++t) { accq[t][0] = z4; accq[t][1] = z4; }
      constexpr int FD = 6, NF = 96;
      auto faddr = [&](int i) -> unsigned { return i < 64 ? (i & 3) * LU + (i >> 2) * 1024 : (4 + ((i - 64) & 1)) * LU + ((i - 64) >> 1) * 1024; };
      v4u wf[FD];
#pragma unroll
      for (int i = 0; i < FD; ++i) wf[i] = ldsf(lb, faddr(i));
      SCHED();
      Stats st;
      KvEpi ke;
      static_for<NF>([&](auto I) {
        constexpr int i = I;
        const v4u f = wf[i % FD];
        if constexpr (i + FD < NF) wf[i % FD] = ldsf(lb, faddr(i + FD));
        if constexpr (i < 64) {
          constexpr int t = i & 3, ks = i >> 2;
          acc[t][0] = mfma(xa[ks], f, acc[t][0]); acc[t][1] = mfma(xb[ks], f, acc[t][1]);
          if constexpr ((i & 3) == 1 && ks + AD < 16) { xa[ks + AD] = ldb1(rs, ab + (ks + AD) * 1024); xb[ks + AD] = ldb1(rs, ab + (16 + ks + AD) * 1024); }
          if constexpr (i == 52) {
            make_stats(sp, st);
            if (q == 0) {
              *(LAS f32x2*)(L + L_STX + w * 256 + n * 8) = f32x2{st.a[0], st.b[0]};
              *(LAS f32x2*)(L + L_STX + w * 256 + 128 + n * 8) = f32x2{st.a[1], st.b[1]};
            }
          }
          if constexpr (i == 60) ke.load(L + L_STX + w * 256, (LAS float*)(L + L_CSQ), lane);
        } else {
          constexpr int t = (i - 64) & 1, ks = (i - 64) >> 1;
          accq[t][0] = mfma(f, xa[ks], accq[t][0]); accq[t][1] = mfma(f, xb[ks], accq[t][1]);
          ke.template step<i - 64>(acc);
        }
        SCHED();
      });
      f32x4 kva, kvb, za, zb;
      ke.finish(kva, kvb, za, zb);
      kv_publish<true>(L, L_KVX, rs, OFF_KVP + (unsigned)((16 + j) * 16 + g) * KVREC, lane, w, kva, kvb, za, zb);
      STAMP(SB + 14);
      publish(flags, FL_KV + j, tid);
      STAMP(SB + 15);
      q_epilogue(accq, st, (LAS float*)(L + L_CSQ), lane, pq);
    }
  });
  STAMP_FLUSH(8, 64);
}

}

extern "C" void kernel_launch(void* const* d_in, const int* in_sizes, int n_in, void* d_out, int out_size, void* d_ws, size_t ws_size,
                              hipStream_t stream) {
  const u16* x = (const u16*)d_in[0];
  const u16* wqkv = (const u16*)d_in[1];
  const u16* wo = (const u16*)d_in[2];
  const u16* w1 = (const u16*)d_in[4];
  const u16* w2 = (const u16*)d_in[5];
  (void)hipFuncSetAttribute((const void*)k1, hipFuncAttributeMaxDynamicSharedMemorySize, K1_LDS);
  (void)hipFuncSetAttribute((const void*)k2, hipFuncAttributeMaxDynamicSharedMemorySize, K2_LDS);
  k1<<<256, 256, K1_LDS, stream>>>(x, wqkv, wo, w1, w2, (unsigned char*)d_ws);
  k2<<<256, 256, K2_LDS, stream>>>(x, (u16*)d_out, (unsigned char*)d_ws);
}
```

```cpp
#include <hip/hip_runtime.h>
#include <stdint.h>
#include <utility>

namespace {

typedef unsigned v4u __attribute__((ext_vector_type(4)));
typedef unsigned v2u __attribute__((ext_vector_type(2)));
typedef float f32x4 __attribute__((ext_vector_type(4)));
typedef float f32x2 __attribute__((ext_vector_type(2)));
typedef __bf16 bf2 __attribute__((ext_vector_type(2)));
typedef __bf16 bf8 __attribute__((ext_vector_type(8)));
typedef short s4v __attribute__((ext_vector_type(4)));
typedef unsigned short u16;
#define DI __device__ __forceinline__
#define GAS __attribute__((address_space(1)))
#define LAS __attribute__((address_space(3)))
typedef GAS unsigned gu32;
#define RLX_AGENT __ATOMIC_RELAXED, __HIP_MEMORY_SCOPE_AGENT
typedef __amdgpu_buffer_rsrc_t rsrc_t;

constexpr unsigned OFF_FLAGS = 0;
constexpr unsigned OFF_WQKV = 100352;
constexpr unsigned OFF_WO = OFF_WQKV + 1536u * 1024u;
constexpr unsigned OFF_W1 = OFF_WO + 512u * 1024u;
constexpr unsigned OFF_W2 = OFF_W1 + 1792u * 1024u;
constexpr unsigned OFF_CS1 = OFF_W2 + 1792u * 1024u;
constexpr unsigned OFF_CSQ = OFF_CS1 + 8u * 1792u * 4u;
constexpr unsigned OFF_PQ = OFF_CSQ + 16u * 96u * 4u;
constexpr unsigned KVREC = 2304;
constexpr unsigned OFF_KVP = OFF_PQ + 256u * 8192u;
constexpr unsigned OFF_ST = OFF_KVP + 2u * 256u * KVREC;
constexpr unsigned ST_BYTES = 128u * 1024u;
constexpr unsigned OFF_O = OFF_ST + 3u * ST_BYTES;
constexpr unsigned XF_BYTES = 128u * 16u * 1024u;
constexpr unsigned OFF_X1 = OFF_O + 2u * XF_BYTES;
constexpr unsigned OFF_X2 = OFF_X1 + 2u * XF_BYTES;
constexpr unsigned HF_BYTES = 128u * 56u * 1024u;
constexpr unsigned OFF_H = OFF_X2 + XF_BYTES;
constexpr unsigned WS_BYTES = OFF_H + 2u * HF_BYTES;
static_assert(OFF_O % 256 == 0 && OFF_KVP % 256 == 0 && OFF_PQ % 256 == 0 && OFF_ST % 256 == 0, "align");

constexpr unsigned FO_PRE = 0, FO_EPOCH = 1024, FO_TMO = 1088, FO_CNT = 2048;
constexpr int FC_O = 0;
constexpr int FC_X1 = 128;
constexpr int FC_H = 384;
constexpr int FC_X2 = 512;
constexpr int FC_KV = 640;
constexpr unsigned FLAG_BYTES = 2048 + 256 * 384;
static_assert(FO_CNT + (FC_KV + 64) * 128 <= FLAG_BYTES, "flags");
constexpr unsigned SPIN_CAP = 1u << 17;

constexpr int AUX_SC1 = 16;

constexpr unsigned LU = 16384;
constexpr unsigned L_SCR = 9 * LU;
constexpr unsigned L_CS1 = L_SCR;
constexpr unsigned L_CSQ = L_SCR + 512;
constexpr unsigned L_KVX = L_SCR + 1024;
constexpr unsigned L_CSP = L_SCR + 9728;
constexpr unsigned L_STX = L_SCR + 11264;
constexpr unsigned L_STAMP = L_SCR + 12288;
constexpr unsigned L_XB = 6 * LU;
constexpr int W1U[2][7] = {{4, 5, 6, 7, 8, 0, 1}, {0, 1, 8, 6, 7, 2, 3}};
constexpr int WOU[2] = {0, 2};
constexpr int QU = 2;
constexpr int REDU[2][3] = {{0, 1, 8}, {4, 5, 6}};
constexpr unsigned K2_LDS = 10 * LU;

DI f32x4 mfma(v4u a, v4u b, f32x4 c) {
  return __builtin_amdgcn_mfma_f32_16x16x32_bf16(__builtin_bit_cast(bf8, a), __builtin_bit_cast(bf8, b), c, 0, 0, 0);
}
template <bool FIRST> DI void mfma2(f32x4& c0, f32x4& c1, v4u a0, v4u b0, v4u a1, v4u b1) {
  if constexpr (FIRST)
    asm volatile("v_mfma_f32_16x16x32_bf16 %0, %2, %3, 0\n\tv_mfma_f32_16x16x32_bf16 %1, %4, %5, 0"
                 : "=&v"(c0), "=&v"(c1) : "v"(a0), "v"(b0), "v"(a1), "v"(b1));
  else
    asm volatile("v_mfma_f32_16x16x32_bf16 %0, %2, %3, %0\n\tv_mfma_f32_16x16x32_bf16 %1, %4, %5, %1"
                 : "+v"(c0), "+v"(c1) : "v"(a0), "v"(b0), "v"(a1), "v"(b1));
}
template <bool FIRST> DI void mfma1(f32x4& c0, v4u a0, v4u b0) {
  if constexpr (FIRST) asm volatile("v_mfma_f32_16x16x32_bf16 %0, %1, %2, 0" : "=&v"(c0) : "v"(a0), "v"(b0));
  else asm volatile("v_mfma_f32_16x16x32_bf16 %0, %1, %2, %0" : "+v"(c0) : "v"(a0), "v"(b0));
}
template <bool FIRST> DI void mfma4a(f32x4 (&c)[4], v4u w0, v4u w1, v4u w2, v4u w3, v4u b) {
  if constexpr (FIRST)
    asm volatile("s_nop 1\n\tv_mfma_f32_16x16x32_bf16 %0, %4, %8, 0\n\tv_mfma_f32_16x16x32_bf16 %1, %5, %8, 0\n\t"
                 "v_mfma_f32_16x16x32_bf16 %2, %6, %8, 0\n\tv_mfma_f32_16x16x32_bf16 %3, %7, %8, 0"
                 : "=&v"(c[0]), "=&v"(c[1]), "=&v"(c[2]), "=&v"(c[3]) : "a"(w0), "a"(w1), "a"(w2), "a"(w3), "v"(b));
  else
    asm volatile("s_nop 1\n\tv_mfma_f32_16x16x32_bf16 %0, %4, %8, %0\n\tv_mfma_f32_16x16x32_bf16 %1, %5, %8, %1\n\t"
                 "v_mfma_f32_16x16x32_bf16 %2, %6, %8, %2\n\tv_mfma_f32_16x16x32_bf16 %3, %7, %8, %3"
                 : "+v"(c[0]), "+v"(c[1]), "+v"(c[2]), "+v"(c[3]) : "a"(w0), "a"(w1), "a"(w2), "a"(w3), "v"(b));
}
#define MFMA_FENCE() asm volatile("s_nop 15\n\ts_nop 3" ::: "memory")
DI void pinv(f32x4& x) { asm volatile("" : "+v"(x)); }
#define PINV(x) pinv(x)
DI unsigned pack2(float a, float b) {
  f32x2 v = {a, b};
  return __builtin_bit_cast(unsigned, __builtin_convertvector(v, bf2));
}
DI float bflo(unsigned u) { return __builtin_bit_cast(float, u << 16); }
DI float bfhi(unsigned u) { return __builtin_bit_cast(float, u & 0xffff0000u); }
DI float dot2(unsigned a, unsigned b, float c) {
  return __builtin_amdgcn_fdot2_f32_bf16(__builtin_bit_cast(bf2, a), __builtin_bit_cast(bf2, b), c, false);
}
constexpr float LOG2E = 1.4426950408889634f;
DI float phi(float t) { return __builtin_amdgcn_exp2f(fminf(t, 0.f) * LOG2E) + (fmaxf(t, 0.f) + 1e-6f); }
DI float silu(float t) { return t * __builtin_amdgcn_rcpf(1.0f + __builtin_amdgcn_exp2f(t * -LOG2E)); }
DI v4u ldb(rsrc_t rs, unsigned off) { return __builtin_amdgcn_raw_buffer_load_b128(rs, off, 0, 0); }
DI v4u ldb1(rsrc_t rs, unsigned off) { return __builtin_amdgcn_raw_buffer_load_b128(rs, off, 0, AUX_SC1); }
DI v4u lda128(v4u srd, unsigned off) {
  v4u t;
  asm volatile("buffer_load_dwordx4 %0, %1, %2, 0 offen sc1" : "=a"(t) : "v"(off), "s"(srd) : "memory");
  return t;
}
DI void stb1(rsrc_t rs, unsigned off, v4u v) { __builtin_amdgcn_raw_buffer_store_b128(v, rs, off, 0, AUX_SC1); }
DI void stb1_64(rsrc_t rs, unsigned off, v2u v) { __builtin_amdgcn_raw_buffer_store_b64(v, rs, off, 0, AUX_SC1); }
#define DRAIN() asm volatile("s_waitcnt vmcnt(0)" ::: "memory")
#define LGKM0() asm volatile("s_waitcnt lgkmcnt(0)" ::: "memory")
#define BAR() do { asm volatile("s_waitcnt lgkmcnt(0)" ::: "memory"); __builtin_amdgcn_s_barrier(); asm volatile("" ::: "memory"); } while (0)
#define SCHED() __builtin_amdgcn_sched_barrier(0)
#define PIN(x) asm volatile("" : "+v"(x))
#define PIN4(a, o) asm volatile("" : "+v"(a[(o)]), "+v"(a[(o) + 1]), "+v"(a[(o) + 2]), "+v"(a[(o) + 3]))
#define STAMP(i) do {} while (0)
#define STAMP_INIT() do {} while (0)
#define STAMP_FLUSH(lo, hi) do {} while (0)

template <int... I, typename F> DI void static_for_impl(std::integer_sequence<int, I...>, F&& f) { (f(std::integral_constant<int, I>{}), ...); }
template <int N, typename F> DI void static_for(F&& f) { static_for_impl(std::make_integer_sequence<int, N>{}, f); }

DI void dma16(const void* sbase, unsigned voff, unsigned lds_dst) {
  unsigned keep;
  asm volatile("s_mov_b32 %0, m0\n\ts_mov_b32 m0, %3\n\ts_nop 0\n\tglobal_load_lds_dwordx4 %1, %2\n\ts_mov_b32 m0, %0"
               : "=&s"(keep) : "v"(voff), "s"(sbase), "s"(lds_dst) : "memory");
}
DI v4u tr_frag(LAS unsigned char* p) {
  s4v lo = __builtin_amdgcn_ds_read_tr16_b64_v4i16((LAS s4v*)(p));
  s4v hi = __builtin_amdgcn_ds_read_tr16_b64_v4i16((LAS s4v*)(p + 256));
  v2u l2 = __builtin_bit_cast(v2u, lo), h2 = __builtin_bit_cast(v2u, hi);
  return v4u{l2.x, l2.y, h2.x, h2.y};
}
DI float sum16(float v) {
  const unsigned u = __builtin_bit_cast(unsigned, v);
  unsigned u2 = u; PIN(u2);
  auto r = __builtin_amdgcn_permlane16_swap(u, u2, false, false);
  const unsigned a = r[0], b = r[1];
  return __builtin_bit_cast(float, a) + __builtin_bit_cast(float, b);
}
DI float sum32(float v) {
  const unsigned u = __builtin_bit_cast(unsigned, v);
  unsigned u2 = u; PIN(u2);
  auto r = __builtin_amdgcn_permlane32_swap(u, u2, false, false);
  const unsigned a = r[0], b = r[1];
  return __builtin_bit_cast(float, a) + __builtin_bit_cast(float, b);
}
DI float sum_q(float v) { return sum32(sum16(v)); }
DI v4u pair16(unsigned a0, unsigned a1, unsigned b0, unsigned b1) {
  auto r0 = __builtin_amdgcn_permlane16_swap(a0, b0, false, false);
  auto r1 = __builtin_amdgcn_permlane16_swap(a1, b1, false, false);
  const unsigned x0 = r0[0], x1 = r1[0], x2 = r0[1], x3 = r1[1];
  return v4u{x0, x1, x2, x3};
}

DI bool spin_fail(unsigned& sp, gu32* fb, unsigned epoch) {
  if (++sp > SPIN_CAP) { __hip_atomic_store(fb + FO_TMO / 4, epoch, RLX_AGENT); return true; }
  return (sp & 255u) == 0u && __hip_atomic_load(fb + FO_TMO / 4, RLX_AGENT) == epoch;
}
DI void poll_ge(gu32* fb, int line, unsigned want, unsigned epoch) {
  gu32* wd = fb + (FO_CNT / 4) + line * 32;
  for (unsigned sp = 0;;) {
    if ((unsigned)__builtin_amdgcn_readfirstlane(__hip_atomic_load(wd, RLX_AGENT)) >= want) return;
    __builtin_amdgcn_s_sleep(1);
    if (spin_fail(sp, fb, epoch)) return;
  }
}
DI void poll_ge2(gu32* fb, int line, unsigned want, unsigned epoch, int lane) {
  gu32* wd = fb + (FO_CNT / 4) + line * 32 + (lane & 1);
  for (unsigned sp = 0;;) {
    if (__all(__hip_atomic_load(wd, RLX_AGENT) >= want)) return;
    __builtin_amdgcn_s_sleep(1);
    if (spin_fail(sp, fb, epoch)) return;
  }
}
DI void arrive(gu32* fb, int line, int dw, int lane) { if (lane == 0) __hip_atomic_fetch_add(fb + (FO_CNT / 4) + line * 32 + dw, 1u, RLX_AGENT); }

struct Stats { float a[2], b[2]; };
DI void finish_stats(float s1, float s2, float& a, float& b) {
  const float mean = s1 * (1.0f / 512.0f);
  const float var = fmaxf(s2 * (1.0f / 512.0f) - mean * mean, 0.f);
  a = __builtin_amdgcn_rsqf(var + 1e-5f);
  b = -mean * a;
}

DI void kv_epilogue(const f32x4 (&acc)[6][2], LAS unsigned char* stx, LAS float* csq, int lane, f32x4& kva, f32x4& kvb, f32x4& za, f32x4& zb) {
  const int q = lane >> 4, n = lane & 15;
  f32x4 ab[2][2];
#pragma unroll
  for (int rt = 0; rt < 2; ++rt) { ab[rt][0] = *(LAS f32x4*)(stx + rt * 128 + q * 32); ab[rt][1] = *(LAS f32x4*)(stx + rt * 128 + q * 32 + 16); }
  v4u PK[2], V[2];
#pragma unroll
  for (int h = 0; h < 2; ++h) {
    const float ck = csq[(2 * h) * 16 + n], cv = csq[(2 * h + 1) * 16 + n];
    unsigned pk[4], pv[4];
#pragma unroll
    for (int rt = 0; rt < 2; ++rt) {
      float a[4], b[4];
#pragma unroll
      for (int r = 0; r < 4; ++r) {
        const float sa = ab[rt][r >> 1][(r & 1) * 2], sb = ab[rt][r >> 1][(r & 1) * 2 + 1];
        a[r] = phi(fmaf(acc[2 * h][rt][r], sa, sb * ck));
        b[r] = fmaf(acc[2 * h + 1][rt][r], sa, sb * cv);
      }
      pk[2 * rt] = pack2(a[0], a[1]); pk[2 * rt + 1] = pack2(a[2], a[3]);
      pv[2 * rt] = pack2(b[0], b[1]); pv[2 * rt + 1] = pack2(b[2], b[3]);
    }
    PK[h] = v4u{pk[0], pk[1], pk[2], pk[3]};
    V[h] = v4u{pv[0], pv[1], pv[2], pv[3]};
  }
  const f32x4 z4 = {0, 0, 0, 0};
  const v4u ones = {0x3f803f80u, 0x3f803f80u, 0x3f803f80u, 0x3f803f80u};
  kva = mfma(PK[0], V[0], z4); kvb = mfma(PK[1], V[1], z4);
  za = mfma(PK[0], ones, z4); zb = mfma(PK[1], ones, z4);
}
DI void q_epilogue(const f32x4 (&acc)[6][2], const Stats& st, LAS float* csq, int lane, v4u (&pq)[2]) {
  const int q = lane >> 4;
#pragma unroll
  for (int rt = 0; rt < 2; ++rt) {
    unsigned p[4];
#pragma unroll
    for (int t = 0; t < 2; ++t) {
      const f32x4 c = *(LAS f32x4*)(csq + (4 + t) * 16 + 4 * q);
      float v[4];
#pragma unroll
      for (int r = 0; r < 4; ++r) v[r] = phi(fmaf(acc[4 + t][rt][r], st.a[rt], st.b[rt] * c[r]));
      p[2 * t] = pack2(v[0], v[1]); p[2 * t + 1] = pack2(v[2], v[3]);
    }
    pq[rt] = v4u{p[0], p[1], p[2], p[3]};
  }
}

DI void kv_publish(LAS unsigned char* L, unsigned kvx, rsrc_t rs, unsigned rec_off, int lane, int w, f32x4 kva, f32x4 kvb, f32x4 za, f32x4 zb) {
  LAS unsigned char* me = L + kvx + w * 2176;
  *(LAS f32x4*)(me + lane * 16) = kva;
  *(LAS f32x4*)(me + 1024 + lane * 16) = kvb;
  if ((lane & 15) == 0) {
    *(LAS f32x4*)(me + 2048 + (lane >> 4) * 16) = za;
    *(LAS f32x4*)(me + 2112 + (lane >> 4) * 16) = zb;
  }
  BAR();
  if (w < 2 || (w == 2 && lane < 8)) {
    const unsigned o = w * 1024 + lane * 16;
    f32x4 s = *(LAS f32x4*)(L + kvx + o);
#pragma unroll
    for (int ww = 1; ww < 4; ++ww) s += *(LAS f32x4*)(L + kvx + ww * 2176 + o);
    stb1(rs, rec_off + o, __builtin_bit_cast(v4u, s));
  }
}

struct LBase { LAS unsigned char* b[3]; };
DI v4u ldsf(const LBase& lb, unsigned off) { return *(LAS v4u*)(lb.b[off >> 16] + (off & 0xffffu)); }

DI void publish(gu32* fb, int line, int tid) {
  DRAIN();
  BAR();
  arrive(fb, line, 0, tid);
}
template <int NKB>
DI void dma_block(const unsigned char* ws, unsigned src, unsigned ldst, int w, int lane) {
  static_assert(NKB % 16 == 0, "");
  const unsigned char* p = ws + src + (unsigned)w * (NKB / 4) * 1024u;
  unsigned d = ldst + (unsigned)w * (NKB / 4) * 1024u;
#pragma unroll 1
  for (int i = 0; i < NKB / 16; ++i) {
    dma16(p, (unsigned)lane * 16u, d);
    dma16(p + 1024, (unsigned)lane * 16u, d + 1024);
    dma16(p + 2048, (unsigned)lane * 16u, d + 2048);
    dma16(p + 3072, (unsigned)lane * 16u, d + 3072);
    p += 4096; d += 4096;
  }
}
template <int NKB>
DI void dma_w123(const unsigned char* ws, unsigned src, unsigned ldst, int w, int lane) {
  if (w == 0) return;
  const unsigned char* p = ws + src + (unsigned)(w - 1) * 1024u;
  unsigned d = ldst + (unsigned)(w - 1) * 1024u;
#pragma unroll 1
  for (int i = w - 1; i < NKB; i += 3) {
    dma16(p, (unsigned)lane * 16u, d);
    p += 3072; d += 3072;
  }
}
template <int NT, int NTN, int AD, int FD, typename FA, typename HOOK>
DI void gemm_k(rsrc_t rs, unsigned ab, const LBase& lb, f32x4 (&acc)[NT][2], FA faddr, HOOK hook) {
  constexpr int R = AD + 1, NF = 16 * NT;
  v4u xa[R], xb[R];
#pragma unroll
  for (int ks = 0; ks < AD; ++ks) { xa[ks] = ldb1(rs, ab + ks * 1024); xb[ks] = ldb1(rs, ab + (16 + ks) * 1024); }
  v4u wf[FD];
#pragma unroll
  for (int i = 0; i < FD; ++i) wf[i] = ldsf(lb, faddr(i % NT, i / NT));
  SCHED();
  static_for<NF>([&](auto I) {
    constexpr int i = I, t = i % NT, ks = i / NT;
    const v4u f = wf[i % FD];
    if constexpr (i + FD < NF) wf[i % FD] = ldsf(lb, faddr((i + FD) % NT, (i + FD) / NT));
    if constexpr (t < NTN) mfma2<ks == 0>(acc[t][0], acc[t][1], xa[ks % R], f, xb[ks % R], f);
    else mfma2<ks == 0>(acc[t][0], acc[t][1], f, xa[ks % R], f, xb[ks % R]);
    if constexpr (t == 1 && ks + AD < 16) { xa[(ks + AD) % R] = ldb1(rs, ab + (ks + AD) * 1024); xb[(ks + AD) % R] = ldb1(rs, ab + (16 + ks + AD) * 1024); }
    hook(I);
    SCHED();
  });
  MFMA_FENCE();
#pragma unroll
  for (int t = 0; t < NT; ++t) { PINV(acc[t][0]); PINV(acc[t][1]); }
}
DI void res_update(v2u (&res)[4], const f32x4 (&acc)[4]) {
#pragma unroll
  for (int c = 0; c < 4; ++c) {
    const unsigned t0 = pack2(acc[c][0], acc[c][1]), t1 = pack2(acc[c][2], acc[c][3]);
    res[c].x = pack2(bflo(res[c].x) + bflo(t0), bfhi(res[c].x) + bfhi(t0));
    res[c].y = pack2(bflo(res[c].y) + bflo(t1), bfhi(res[c].y) + bfhi(t1));
  }
}
DI void store_x_tile(rsrc_t rs, unsigned xoff, unsigned stoff, const v2u (&res)[4], int myrt, int cs, int lane) {
  const int q = lane >> 4, n = lane & 15;
  const unsigned kg = (q & 1) * 2 + (q >> 1);
#pragma unroll
  for (int c2 = 0; c2 < 2; ++c2) {
    const v4u o = pair16(res[2 * c2].x, res[2 * c2].y, res[2 * c2 + 1].x, res[2 * c2 + 1].y);
    stb1(rs, xoff + (unsigned)(myrt * 16 + 2 * cs + c2) * 1024u + (kg * 16 + n) * 16, o);
  }
  float s1 = 0, s2 = 0;
#pragma unroll
  for (int c = 0; c < 4; ++c) {
    s1 = dot2(res[c].x, 0x3f803f80u, s1); s2 = dot2(res[c].x, res[c].x, s2);
    s1 = dot2(res[c].y, 0x3f803f80u, s1); s2 = dot2(res[c].y, res[c].y, s2);
  }
  s1 = sum_q(s1); s2 = sum_q(s2);
  if (q == 0) stb1_64(rs, stoff + (unsigned)myrt * 1024u + n * 64 + cs * 8, __builtin_bit_cast(v2u, f32x2{s1, s2}));
}
DI void load_stats(rsrc_t rs, unsigned stoff, int rt0, int lane, v4u (&sp)[2]) {
  const int q = lane >> 4, n = lane & 15;
#pragma unroll
  for (int rt = 0; rt < 2; ++rt) sp[rt] = ldb1(rs, stoff + (unsigned)(rt0 + rt) * 1024u + n * 64 + q * 16);
}
DI void make_stats(const v4u (&sp)[2], Stats& st) {
#pragma unroll
  for (int rt = 0; rt < 2; ++rt) {
    const f32x4 p = __builtin_bit_cast(f32x4, sp[rt]);
    finish_stats(sum_q(p[0] + p[2]), sum_q(p[1] + p[3]), st.a[rt], st.b[rt]);
  }
}

__global__ void __launch_bounds__(256, 1) kmain(const u16* __restrict__ x, const u16* __restrict__ wqkv, const u16* __restrict__ wo,
                                                const u16* __restrict__ w1, const u16* __restrict__ w2g, u16* __restrict__ out,
                                                unsigned char* __restrict__ ws) {
  extern __shared__ __attribute__((aligned(16))) unsigned char smem[];
  LAS unsigned char* L = (LAS unsigned char*)smem;
  constexpr unsigned LSTAMP = L_STAMP;
  const unsigned lbase = (unsigned)(size_t)L;
  const int tid = threadIdx.x, lane0 = tid & 63, w = __builtin_amdgcn_readfirstlane(tid >> 6);
  const int b = blockIdx.x;
  const rsrc_t rs = __builtin_amdgcn_make_buffer_rsrc((void*)ws, 0, WS_BYTES, 0x00020000);
  const v4u srd = {(unsigned)(size_t)ws, (unsigned)((size_t)ws >> 32) & 0xffffu, WS_BYTES, 0x00020000u};
  gu32* fb = (gu32*)(ws + OFF_FLAGS);
  const f32x4 z4 = {0, 0, 0, 0};
  const int gm = 2 * (b & 7) + ((b >> 3) >> 4), jm = (b >> 3) & 15;
  const int hh = jm >> 3, cs = jm & 7;
#define FRESH() int lane = lane0; PIN(lane); const int q = lane >> 4, n = lane & 15; const unsigned l16 = (unsigned)lane * 16u; \
  LBase lb; { unsigned o1 = l16 + 65536u, o2 = l16 + 131072u; PIN(o1); PIN(o2); lb.b[0] = L + l16; lb.b[1] = L + o1; lb.b[2] = L + o2; } (void)q; (void)n; (void)lb; \
  const unsigned char* wsp = ws; asm volatile("" : "+s"(wsp)); int jo = jm; asm volatile("" : "+s"(jo)); (void)wsp; (void)jo;
  STAMP_INIT();
  STAMP(0);
  const unsigned epoch = __hip_atomic_load(fb + FO_EPOCH / 4, RLX_AGENT) + 1u;

  v4u w2[14][4];
  auto ldw2 = [&](auto I, unsigned l16) {
    constexpr int i = I;
    w2[i >> 2][i & 3] = ldb1(rs, OFF_W2 + (unsigned)((cs * 4 + w) * 56 + i) * 1024u + l16);
  };
  {
    FRESH();
    const int g = 4 * ((b & 7) >> 1) + (b >> 6), j = 8 * (b & 1) + ((b >> 3) & 7);
    const int drow = lane >> 2, dch = (lane & 3) ^ (((drow >> 3) & 1) << 1);
    {
      const unsigned voff = (unsigned)(drow * 1536 + 32 * j + dch * 8) * 2u;
#pragma unroll
      for (int i = 0; i < 24; ++i) {
        const int it = 4 * i + w, rb = it / 3, seg = it % 3;
        dma16((const unsigned char*)wqkv + (size_t)(rb * 16 * 1536 + seg * 512) * 2, voff, lbase + (seg * 32 + rb) * 1024);
      }
    }
    v4u xr[4][8];
    {
      const GAS v4u* xp = (const GAS v4u*)(x + (size_t)(128 * g + 32 * w + (lane >> 5)) * 512 + (lane & 31) * 8);
#pragma unroll
      for (int blk = 0; blk < 4; ++blk)
#pragma unroll
        for (int i = 0; i < 8; ++i) xr[blk][i] = xp[((blk >> 1) * 16 + 2 * i) * 64 + (blk & 1) * 32];
    }
    const u16* psrc; int pld, pp, pks, pkind, prow[4];
    if (b < 112) { psrc = w1; pld = 1792; pp = b >> 1; pks = 8 * (b & 1) + 2 * w; pkind = 0; }
    else {
      const int u = (b - 112) * 8 + 2 * w;
      if (u < 896) { psrc = w2g; pld = 512; pp = u / 56; pks = u % 56; pkind = 1; }
      else { psrc = wo; pld = 512; pp = (u - 896) >> 4; pks = (u - 896) & 15; pkind = 2; }
    }
#pragma unroll
    for (int i = 0; i < 4; ++i) {
      const int ks = pks + (i >> 1);
      prow[i] = pkind == 1 ? 112 * (2 * (ks / 7) + (i & 1)) + 16 * (ks % 7) : 32 * ks + 16 * (i & 1);
    }
    v4u pt[4];
#pragma unroll
    for (int i = 0; i < 4; ++i) pt[i] = *(const GAS v4u*)(psrc + (size_t)(prow[i] + drow) * pld + 32 * pp + dch * 8);
    SCHED();
    if (tid < 24) stb1(rs, OFF_FLAGS + FO_CNT + b * 384 + tid * 16, v4u{0u, 0u, 0u, 0u});
    STAMP(1);

    v4u xa[16], xb[16];
    float s1a = 0, s2a = 0, s1b = 0, s2b = 0;
    LAS unsigned char* B = L + L_XB + w * 8192;
    {
      const int c = lane & 31, ksl = c >> 2, kgw = c & 3, fw = (ksl & 1) * 4 + kgw;
      LAS unsigned char* wp = B + ksl * 1024 + kgw * 256;
      LAS unsigned char* rp0 = B + q * 256 + ((n ^ q) * 16);
      LAS unsigned char* rp1 = B + q * 256 + ((n ^ (4 + q)) * 16);
#pragma unroll
      for (int blk = 0; blk < 4; ++blk) {
#pragma unroll
        for (int i = 0; i < 8; ++i) *(LAS v4u*)(wp + (((2 * i + (lane >> 5)) ^ fw) * 16)) = xr[blk][i];
        LGKM0();
#pragma unroll
        for (int k8 = 0; k8 < 8; ++k8) {
          const v4u f = *(LAS v4u*)(((k8 & 1) ? rp1 : rp0) + k8 * 1024);
          if (blk < 2) xa[(blk & 1) * 8 + k8] = f; else xb[(blk & 1) * 8 + k8] = f;
#pragma unroll
          for (int e = 0; e < 4; ++e) {
            if (blk < 2) { s1a = dot2(f[e], 0x3f803f80u, s1a); s2a = dot2(f[e], f[e], s2a); }
            else { s1b = dot2(f[e], 0x3f803f80u, s1b); s2b = dot2(f[e], f[e], s2b); }
          }
        }
        PIN(s1a); PIN(s2a); PIN(s1b); PIN(s2b);
        LGKM0();
      }
    }
    STAMP(2);
    const int r4 = n >> 2, c4 = n & 3;
    unsigned trb[2];
#pragma unroll
    for (int ct = 0; ct < 2; ++ct)
      trb[ct] = (unsigned)((q >> 1) * 1024 + ((q & 1) * 8 + r4) * 64 + (2 * (ct ^ (q & 1)) + (c4 >> 1)) * 16 + (c4 & 1) * 8);
    const v4u ones = {0x3f803f80u, 0x3f803f80u, 0x3f803f80u, 0x3f803f80u};
    {
#pragma unroll
      for (int i = 0; i < 4; ++i) *(LAS v4u*)(B + i * 1024 + l16) = pt[i];
      LGKM0();
      f32x4 c1[2] = {z4, z4};
#pragma unroll
      for (int u = 0; u < 2; ++u)
#pragma unroll
        for (int ct = 0; ct < 2; ++ct) {
          const v4u f = tr_frag(B + u * 2048 + trb[ct]);
          const int gct = 2 * pp + ct, ks = pks + u;
          unsigned dst;
          if (pkind == 0) dst = OFF_W1 + (unsigned)((gct / 7) * 112 + (gct % 7) * 16 + ks) * 1024u;
          else if (pkind == 1) { const int p = ks / 7, t = ks % 7; dst = OFF_W2 + (unsigned)(((gct >> 2) * 4 + (p >> 1)) * 56 + (2 * t + (p & 1)) * 4 + (gct & 3)) * 1024u; }
          else dst = OFF_WO + (unsigned)((gct >> 2) * 64 + (gct & 3) * 16 + ks) * 1024u;
          stb1(rs, dst + l16, f);
          c1[ct] = mfma(f, ones, c1[ct]);
        }
      if (pkind == 0 && n == 0) {
        const int part = (b & 1) * 4 + w;
#pragma unroll
        for (int ct = 0; ct < 2; ++ct)
          stb1(rs, OFF_CS1 + (unsigned)(part * 1792 + 32 * pp + 16 * ct + 4 * q) * 4u, __builtin_bit_cast(v4u, c1[ct]));
      }
    }
    Stats st;
    finish_stats(sum_q(s1a), sum_q(s2a), st.a[0], st.b[0]);
    finish_stats(sum_q(s1b), sum_q(s2b), st.a[1], st.b[1]);
    if (q == 0) {
      *(LAS f32x2*)(L + L_STX + w * 256 + n * 8) = f32x2{st.a[0], st.b[0]};
      *(LAS f32x2*)(L + L_STX + w * 256 + 128 + n * 8) = f32x2{st.a[1], st.b[1]};
    }
    STAMP(3);
    DRAIN();
    BAR();
    STAMP(4);
    {
      f32x4 cs[6] = {z4, z4, z4, z4, z4, z4};
#pragma unroll
      for (int i = 0; i < 4; ++i)
#pragma unroll
        for (int seg = 0; seg < 3; ++seg) {
          LAS unsigned char* blk = L + seg * 32768 + (4 * w + i) * 2048;
          const v4u f0 = tr_frag(blk + trb[0]), f1 = tr_frag(blk + trb[1]);
          const int t0 = seg == 0 ? 4 : seg - 1, t1 = seg == 0 ? 5 : seg + 1;
          if (seg == 0) { cs[4] = mfma(f0, ones, cs[4]); cs[5] = mfma(f1, ones, cs[5]); }
          else { cs[seg - 1] = mfma(ones, f0, cs[seg - 1]); cs[seg + 1] = mfma(ones, f1, cs[seg + 1]); }
          *(LAS v4u*)(blk + l16) = f0;
          *(LAS v4u*)(blk + 1024 + l16) = f1;
          if (4 * w + i == g) {
            stb1(rs, OFF_WQKV + (unsigned)(j * 96 + t0 * 16 + g) * 1024u + l16, f0);
            stb1(rs, OFF_WQKV + (unsigned)(j * 96 + t1 * 16 + g) * 1024u + l16, f1);
          }
        }
      LAS float* csw = (LAS float*)(L + L_CSP) + w * 96;
#pragma unroll
      for (int t = 4; t < 6; ++t) if (n == 0) *(LAS f32x4*)(csw + t * 16 + 4 * q) = cs[t];
#pragma unroll
      for (int t = 0; t < 4; ++t) if (q == 0) csw[t * 16 + n] = cs[t][0];
    }
    BAR();
    if (tid < 96) {
      LAS float* c = (LAS float*)(L + L_CSP);
      const float s = (c[tid] + c[96 + tid]) + (c[192 + tid] + c[288 + tid]);
      ((LAS float*)(L + L_CSQ))[tid] = s;
      if (g == 0) __hip_atomic_store((GAS float*)(ws + OFF_CSQ) + j * 96 + tid, s, RLX_AGENT);
    }
    DRAIN();
    BAR();
    if (tid == 0) __hip_atomic_store(fb + FO_PRE / 4 + b, epoch, RLX_AGENT);
    f32x4 acc[6][2];
    {
      constexpr int FD = 6, NF = 96;
      auto addr = [&](int i) -> unsigned {
        const int t = i % 6, ks = i / 6;
        const int seg = t < 4 ? 1 + (t & 1) : 0, ct = t < 4 ? (t >> 1) : (t - 4);
        return (unsigned)(seg * 32768 + ks * 2048 + ct * 1024);
      };
      v4u wf[FD];
#pragma unroll
      for (int i = 0; i < FD; ++i) wf[i] = ldsf(lb, addr(i));
      SCHED();
      static_for<NF>([&](auto I) {
        constexpr int i = I, t = i % 6, ks = i / 6;
        const v4u f = wf[i % FD];
        if constexpr (i + FD < NF) wf[i % FD] = ldsf(lb, addr(i + FD));
        if constexpr (t < 4) mfma2<ks == 0>(acc[t][0], acc[t][1], xa[ks], f, xb[ks], f);
        else mfma2<ks == 0>(acc[t][0], acc[t][1], f, xa[ks], f, xb[ks]);
        SCHED();
      });
    }
    MFMA_FENCE();
#pragma unroll
    for (int t = 0; t < 6; ++t) { PINV(acc[t][0]); PINV(acc[t][1]); }
    STAMP(5);
    f32x4 kva, kvb, za, zb;
    kv_epilogue(acc, L + L_STX + w * 256, (LAS float*)(L + L_CSQ), lane, kva, kvb, za, zb);
    v4u pq0[2];
    q_epilogue(acc, st, (LAS float*)(L + L_CSQ), lane, pq0);
    const int cu = g * 16 + j;
#pragma unroll
    for (int rt = 0; rt < 2; ++rt) stb1(rs, OFF_PQ + (unsigned)((cu * 4 + w) * 2 + rt) * 1024u + l16, pq0[rt]);
    if (w == 0) {
      for (unsigned sp = 0;;) {
        const v4u v = ldb1(rs, OFF_FLAGS + FO_PRE + l16);
        if (__all(v.x == epoch && v.y == epoch && v.z == epoch && v.w == epoch)) break;
        __builtin_amdgcn_s_sleep(4);
        if (spin_fail(sp, fb, epoch)) break;
      }
    }
    kv_publish(L, L_KVX, rs, OFF_KVP + (unsigned)(j * 16 + g) * KVREC, lane, w, kva, kvb, za, zb);
    STAMP(6);
    publish(fb, FC_KV + 2 * j, tid);
    STAMP(7);
  }

  const int g = gm, j = jm;
  const int cu = g * 16 + j;
  const int myrt = 8 * g + 4 * hh + w;
  const int rt0 = 8 * g + 2 * w;
  v4u pq[2];
  v2u res[4];
  dma_w123<64>(ws, OFF_WO + cs * 65536u, lbase + WOU[0] * LU, w, lane0);
  dma_w123<32>(ws, OFF_W1 + j * 114688u, lbase + W1U[0][0] * LU, w, lane0);
  static_for<14>([&](auto I) { ldw2(I, (unsigned)lane0 * 16u); });
  {
    FRESH();
    const GAS v2u* xp = (const GAS v2u*)(x + (size_t)(16 * myrt + n) * 512 + 64 * cs + 4 * q);
#pragma unroll
    for (int c = 0; c < 4; ++c) res[c] = xp[c * 4];
    if (tid < 112) {
      float csv = 0.f;
#pragma unroll
      for (int i = 0; i < 8; ++i) csv += __hip_atomic_load((GAS float*)(ws + OFF_CS1) + i * 1792 + 112 * j + tid, RLX_AGENT);
      ((LAS float*)(L + L_CS1))[tid] = csv;
    } else if (tid >= 128 && tid < 224) {
      ((LAS float*)(L + L_CSQ))[tid - 128] = __hip_atomic_load((GAS float*)(ws + OFF_CSQ) + j * 96 + tid - 128, RLX_AGENT);
    }
  }
  using std::integral_constant;
#define IC(x) integral_constant<int, (x)>{}
#define VMW(n) asm volatile("s_waitcnt vmcnt(%0)" :: "i"(n) : "memory")

  static_for<2>([&](auto LL) {
    constexpr int l = LL;
    constexpr int SB = 10 + l * 24;
    if (w == 0) poll_ge(fb, FC_KV + (l * 16 + j) * 2, 16u, epoch);
    BAR();
    STAMP(SB + 0);
    {
      FRESH();
      if constexpr (l == 0) {
#pragma unroll
        for (int rt = 0; rt < 2; ++rt) pq[rt] = ldb1(rs, OFF_PQ + (unsigned)((cu * 4 + w) * 2 + rt) * 1024u + l16);
      }
      const unsigned rb = OFF_KVP + (unsigned)((l * 16 + j) * 16) * KVREC;
      if (w < 3) {
        const unsigned o = rb + (w < 2 ? (unsigned)w * 1024u + l16 : 2048u + (unsigned)(lane & 7) * 16u);
        v4u a[16];
#pragma unroll
        for (int i = 0; i < 16; ++i) a[i] = ldb1(rs, o + i * KVREC);
        SCHED();
        f32x4 s[4];
#pragma unroll
        for (int i = 0; i < 4; ++i)
          s[i] = (__builtin_bit_cast(f32x4, a[4 * i]) + __builtin_bit_cast(f32x4, a[4 * i + 1])) + (__builtin_bit_cast(f32x4, a[4 * i + 2]) + __builtin_bit_cast(f32x4, a[4 * i + 3]));
        const f32x4 t = (s[0] + s[1]) + (s[2] + s[3]);
        const v2u pk = v2u{pack2(t[0], t[1]), pack2(t[2], t[3])};
        if (w < 2) *(LAS v2u*)(L + L_KVX + w * 512 + lane * 8) = pk;
        else if (lane < 8) *(LAS v2u*)(L + L_KVX + 1024 + lane * 8) = pk;
      }
      BAR();
      const v2u ka = *(LAS v2u*)(L + L_KVX + lane * 8), kb = *(LAS v2u*)(L + L_KVX + 512 + lane * 8);
      const v2u zz = *(LAS v2u*)(L + L_KVX + 1024 + ((n & 1) * 4 + q) * 8);
      const v4u Aa = {ka.x, ka.y, 0u, 0u};
      const v4u Ab = {0u, 0u, kb.x, kb.y};
      const bool za_ = (n & 3) == 0, zb_ = (n & 3) == 1;
      const v4u Az = {za_ ? zz.x : 0u, za_ ? zz.y : 0u, zb_ ? zz.x : 0u, zb_ ? zz.y : 0u};
#pragma unroll
      for (int rt = 0; rt < 2; ++rt) {
        const f32x4 na = mfma(Aa, pq[rt], z4), nb = mfma(Ab, pq[rt], z4), dn = mfma(Az, pq[rt], z4);
        const float ra = __builtin_amdgcn_rcpf(dn[0] + 1e-6f), rb2 = __builtin_amdgcn_rcpf(dn[1] + 1e-6f);
        const v4u o = pair16(pack2(na[0] * ra, na[1] * ra), pack2(na[2] * ra, na[3] * ra), pack2(nb[0] * rb2, nb[1] * rb2), pack2(nb[2] * rb2, nb[3] * rb2));
        const unsigned kg = (q & 1) * 2 + (q >> 1);
        stb1(rs, OFF_O + l * XF_BYTES + (unsigned)((rt0 + rt) * 16 + j) * 1024u + (kg * 16 + n) * 16, o);
      }
      STAMP(SB + 1);
      DRAIN();
      arrive(fb, FC_O + (l * 16 + g) * 4 + w, 0, lane);
      BAR();
      STAMP(SB + 2);
      if constexpr (l == 0) {
        static_for<12>([&](auto K) {
          const unsigned pc = (unsigned)((K & 3) * 4 + w) * 1024u;
          dma16(wsp + OFF_W1 + jo * 114688u + (2 + (K >> 2)) * LU + pc, l16, lbase + W1U[0][2 + (K >> 2)] * LU + pc);
        });
      } else {
        static_for<8>([&](auto K) {
          const unsigned pc = (unsigned)((K & 3) * 4 + w) * 1024u;
          dma16(wsp + OFF_W1 + jo * 114688u + (3 + (K >> 2)) * LU + pc, l16, lbase + W1U[1][3 + (K >> 2)] * LU + pc);
        });
      }
    }
    poll_ge(fb, FC_O + (l * 16 + g) * 4 + ((4 * hh + w) >> 1), 16u, epoch);
    STAMP(SB + 3);
    {
      FRESH();
      v4u a[16];
      const unsigned ab = OFF_O + l * XF_BYTES + (unsigned)(myrt * 16) * 1024u + l16;
      constexpr int AD = 8;
#pragma unroll
      for (int ks = 0; ks < AD; ++ks) a[ks] = ldb1(rs, ab + ks * 1024);
      f32x4 acc[4];
      constexpr int FD = 8;
      v4u wf[FD];
#pragma unroll
      for (int i = 0; i < FD; ++i) wf[i] = ldsf(lb, (WOU[l] + (i & 3)) * LU + (i >> 2) * 1024);
      SCHED();
      static_for<64>([&](auto I) {
        constexpr int i = I;
        const v4u f = wf[i % FD];
        if constexpr (i + FD < 64) wf[i % FD] = ldsf(lb, (WOU[l] + ((i + FD) & 3)) * LU + ((i + FD) >> 2) * 1024);
        mfma1<(i < 4)>(acc[i & 3], f, a[i >> 2]);
        if constexpr ((i & 3) == 1 && (i >> 2) + AD < 16) a[(i >> 2) + AD] = ldb1(rs, ab + ((i >> 2) + AD) * 1024);
        SCHED();
      });
      MFMA_FENCE();
      PINV(acc[0]); PINV(acc[1]); PINV(acc[2]); PINV(acc[3]);
      res_update(res, acc);
      store_x_tile(rs, OFF_X1 + l * XF_BYTES, OFF_ST + l * ST_BYTES, res, myrt, cs, lane);
      STAMP(SB + 4);
      DRAIN();
      arrive(fb, FC_X1 + (l * 16 + g) * 8 + 4 * hh + w, 0, lane);
      BAR();
      STAMP(SB + 5);
      static_for<8>([&](auto K) {
        const unsigned pc = (unsigned)((K & 3) * 4 + w) * 1024u;
        dma16(wsp + OFF_W1 + jo * 114688u + (5 + (K >> 2)) * LU + pc, l16, lbase + W1U[l][5 + (K >> 2)] * LU + pc);
      });
      if constexpr (l == 0) static_for<14>([&](auto I) { ldw2(IC(14 + I), l16); });
    }

    poll_ge(fb, FC_X1 + (l * 16 + g) * 8 + 2 * w, 8u, epoch);
    poll_ge(fb, FC_X1 + (l * 16 + g) * 8 + 2 * w + 1, 8u, epoch);
    STAMP(SB + 6);
    {
      FRESH();
      const int hline_out = FC_H + ((l * 16 + g) * 2 + (w >> 1)) * 2, hline_in = FC_H + ((l * 16 + g) * 2 + hh) * 2;
      const unsigned ab = OFF_X1 + l * XF_BYTES + (unsigned)(rt0 * 16) * 1024u + l16;
      v4u xf[32], sp[2];
      constexpr int AD = 12, FD = 7, NF = 112;
      load_stats(rs, OFF_ST + l * ST_BYTES, rt0, lane, sp);
#pragma unroll
      for (int f = 0; f < AD; ++f) xf[f] = ldb1(rs, ab + f * 1024);
      VMW(AD + 2);
      BAR();
      auto faddr = [&](int i) -> unsigned { return (unsigned)(W1U[l][i % 7] * LU + (i / 7) * 1024); };
      float sa[2], sb[2];
      const unsigned hst = OFF_H + l * HF_BYTES + ((unsigned)rt0 * 56u + (unsigned)(j >> 1) * 7u) * 1024u + ((2 * (j & 1) + (q >> 1)) * 16 + n) * 16;
      v4u ho[2][4];
      auto half1 = [&](auto HH, auto hook) {
        constexpr int H = HH;
        f32x4 acc[7];
        v4u wf[FD];
#pragma unroll
        for (int i = 0; i < FD; ++i) wf[i] = ldsf(lb, faddr(i));
        SCHED();
        static_for<NF>([&](auto I) {
          constexpr int i = I, t = i % 7, ks = i / 7;
          const v4u f = wf[i % FD];
          if constexpr (i + FD < NF) wf[i % FD] = ldsf(lb, faddr(i + FD));
          mfma1<ks == 0>(acc[t], f, xf[H * 16 + ks]);
          hook(I);
          SCHED();
        });
        MFMA_FENCE();
#pragma unroll
        for (int t = 0; t < 7; ++t) PINV(acc[t]);
        if constexpr (H == 0) {
          Stats st;
          make_stats(sp, st);
          sa[0] = st.a[0] * LOG2E; sa[1] = st.a[1] * LOG2E; sb[0] = st.b[0] * LOG2E; sb[1] = st.b[1] * LOG2E;
        }
        unsigned y2[7][2];
#pragma unroll
        for (int t = 0; t < 7; ++t) {
          const f32x4 c = *(LAS f32x4*)(L + L_CS1 + (t * 16 + 4 * q) * 4);
          float y[4];
#pragma unroll
          for (int r = 0; r < 4; ++r) {
            const float v = fmaf(acc[t][r], sa[H], sb[H] * c[r]);
            y[r] = v * __builtin_amdgcn_rcpf(fmaf(__builtin_amdgcn_exp2f(-v), LOG2E, LOG2E));
          }
          y2[t][0] = pack2(y[0], y[1]); y2[t][1] = pack2(y[2], y[3]);
        }
#pragma unroll
        for (int p = 0; p < 3; ++p) ho[H][p] = pair16(y2[2 * p][0], y2[2 * p][1], y2[2 * p + 1][0], y2[2 * p + 1][1]);
        ho[H][3] = v4u{y2[6][0], y2[6][1], 0u, 0u};
      };
      auto hstore = [&](auto HH) {
        constexpr int H = HH;
#pragma unroll
        for (int p = 0; p < 3; ++p) stb1(rs, hst + H * 56 * 1024 + (2 * p + (q & 1)) * 1024, ho[H][p]);
        stb1_64(rs, hst + H * 56 * 1024 + 6 * 1024 + (q & 1) * 8, v2u{ho[H][3].x, ho[H][3].y});
      };
      half1(IC(0), [&](auto I) {
        constexpr int i = I;
        if constexpr (i % 4 == 2 && AD + i / 4 < 32) xf[AD + i / 4] = ldb1(rs, ab + (AD + i / 4) * 1024);
        if constexpr (i == 0) STAMP(SB + 16);
      });
      STAMP(SB + 17);
      hstore(IC(0));
      SCHED();
      half1(IC(1), [&](auto I) {
        constexpr int i = I;
        if constexpr (i == 72) { VMW(0); arrive(fb, hline_out + 0, w & 1, lane); }
        if constexpr (l == 0 && i >= 76 && i < 104) ldw2(IC(28 + i - 76), l16);
      });
      STAMP(SB + 18);
      constexpr int WIN = 16;
      v4u a[WIN];
      f32x4 acc2[2][4];
      auto hoff = [](int f) -> unsigned { return (unsigned)(((f >> 1) & 1) * 7 + (f >> 2)) * 1024u + (unsigned)(f & 1) * (2 * 56 * 1024); };
      auto hbase = [&](int H) -> unsigned { return OFF_H + l * HF_BYTES + (unsigned)((8 * g + 4 * hh + H) * 56 + 14 * w) * 1024u + l16; };
      auto gemm2_start = [&](auto HH) {
        poll_ge2(fb, hline_in + HH, 16u, epoch, lane);
        const unsigned hb = hbase(HH);
#pragma unroll
        for (int f = 0; f < WIN; ++f) a[f] = ldb1(rs, hb + hoff(f));
      };
      auto gemm2 = [&](auto HH, auto hook) {
        const unsigned hb = hbase(HH);
        static_for<28>([&](auto F) {
          constexpr int f = F, wi = 2 * (f >> 2) + ((f >> 1) & 1);
          const v4u cur = a[f % WIN];
          if constexpr (f + WIN < 28) a[f % WIN] = ldb1(rs, hb + hoff(f + WIN));
          mfma4a<(f < 2)>(acc2[f & 1], w2[wi][0], w2[wi][1], w2[wi][2], w2[wi][3], cur);
          hook(F);
          SCHED();
        });
        MFMA_FENCE();
#pragma unroll
        for (int i2 = 0; i2 < 2; ++i2)
#pragma unroll
          for (int c = 0; c < 4; ++c) PINV(acc2[i2][c]);
      };
      auto reduce2 = [&](auto HH) {
        constexpr int H = HH;
#pragma unroll
        for (int i2 = 0; i2 < 2; ++i2) {
          const int own = H + 2 * i2;
          if (w != own) {
            const int slot = w - (w > own ? 1 : 0);
#pragma unroll
            for (int c = 0; c < 4; ++c) *(LAS f32x4*)(L + REDU[l][i2] * LU + (slot * 4 + c) * 1024 + l16) = acc2[i2][c];
          }
        }
        BAR();
        if ((w & 1) == H) {
          f32x4 s[4];
          const unsigned ru = (w >> 1) ? (unsigned)REDU[l][1] * LU : (unsigned)REDU[l][0] * LU;
#pragma unroll
          for (int c = 0; c < 4; ++c) s[c] = (w >> 1) ? acc2[1][c] : acc2[0][c];
#pragma unroll
          for (int sl = 0; sl < 3; ++sl)
#pragma unroll
            for (int c = 0; c < 4; ++c) s[c] += *(LAS f32x4*)(L + ru + (sl * 4 + c) * 1024 + l16);
          res_update(res, s);
          if constexpr (l == 0) {
            store_x_tile(rs, OFF_X2, OFF_ST + 2 * ST_BYTES, res, myrt, cs, lane);
          } else {
#pragma unroll
            for (int c2 = 0; c2 < 2; ++c2) {
              const v4u o = pair16(res[2 * c2].x, res[2 * c2].y, res[2 * c2 + 1].x, res[2 * c2 + 1].y);
              *(GAS v4u*)(out + (size_t)(16 * myrt + n) * 512 + 64 * cs + 16 * (2 * c2 + (q & 1)) + 8 * (q >> 1)) = o;
            }
          }
        }
      };
      gemm2_start(IC(0));
      hstore(IC(1));
      STAMP(SB + 7);
      BAR();
      gemm2(IC(0), [&](auto F) {
        constexpr int f = F;
        if constexpr (f == 14) { VMW(12); arrive(fb, hline_out + 1, w & 1, lane); }
        if constexpr (l == 0 && f >= 16) {
          const unsigned o = (unsigned)((f - 16) * 4 + w) * 1024u;
          dma16(wsp + OFF_WQKV + jo * 98304u + o, l16, lbase + QU * LU + o);
        }
      });
      STAMP(SB + 9);
      gemm2_start(IC(1));
      reduce2(IC(0));
      STAMP(SB + 10);
      gemm2(IC(1), [&](auto F) {
        constexpr int f = F;
        if constexpr (l == 0 && f == 14) { VMW(12); if ((w & 1) == 0) arrive(fb, FC_X2 + g * 8 + 4 * hh + w, 0, lane); }
        if constexpr (l == 0 && f >= 16) {
          const unsigned o = (unsigned)(48 + (f - 16) * 4 + w) * 1024u;
          dma16(wsp + OFF_WQKV + jo * 98304u + o, l16, lbase + QU * LU + o);
        }
      });
      BAR();
      reduce2(IC(1));
      STAMP(SB + 11);
      if constexpr (l == 0) {
        DRAIN();
        if ((w & 1) == 1) arrive(fb, FC_X2 + g * 8 + 4 * hh + w, 0, lane);
      }
    }
    if constexpr (l == 0) {
      poll_ge(fb, FC_X2 + g * 8 + 2 * w, 8u, epoch);
      BAR();
      STAMP(SB + 13);
      FRESH();
      const unsigned ab = OFF_X2 + (unsigned)(rt0 * 16) * 1024u + l16;
      v4u xf[32], sp[2];
      constexpr int AD = 12, FD = 6, NF = 96;
      sp[0] = ldb1(rs, OFF_ST + 2 * ST_BYTES + (unsigned)rt0 * 1024u + n * 64 + q * 16);
#pragma unroll
      for (int f = 0; f < AD; ++f) xf[f] = ldb1(rs, ab + f * 1024);
      f32x4 acc[6][2];
      Stats st;
      auto halfq = [&](auto HH, auto hook) {
        constexpr int H = HH;
        auto faddr = [&](int i) -> unsigned { return (unsigned)((QU + i % 6) * LU + (i / 6) * 1024); };
        v4u wf[FD];
#pragma unroll
        for (int i = 0; i < FD; ++i) wf[i] = ldsf(lb, faddr(i));
        SCHED();
        static_for<NF>([&](auto I) {
          constexpr int i = I, t = i % 6, ks = i / 6;
          const v4u f = wf[i % FD];
          if constexpr (i + FD < NF) wf[i % FD] = ldsf(lb, faddr(i + FD));
          if constexpr (t < 4) mfma1<ks == 0>(acc[t][H], xf[H * 16 + ks], f);
          else mfma1<ks == 0>(acc[t][H], f, xf[H * 16 + ks]);
          hook(I);
          SCHED();
        });
      };
      halfq(IC(0), [&](auto I) {
        constexpr int i = I;
        if constexpr (i % 12 == 2 && AD + i / 12 * 1 < 16) xf[AD + i / 12] = ldb1(rs, ab + (AD + i / 12) * 1024);
        if constexpr (i == 48) {
          poll_ge(fb, FC_X2 + g * 8 + 2 * w + 1, 8u, epoch);
          sp[1] = ldb1(rs, OFF_ST + 2 * ST_BYTES + (unsigned)(rt0 + 1) * 1024u + n * 64 + q * 16);
        }
        if constexpr (i > 48 && i <= 80 && (i & 1) == 0) xf[16 + (i - 50) / 2] = ldb1(rs, ab + (16 + (i - 50) / 2) * 1024);
      });
      halfq(IC(1), [&](auto I) {
        constexpr int i = I;
        if constexpr (i == 40) {
          make_stats(sp, st);
          if (q == 0) {
            *(LAS f32x2*)(L + L_STX + w * 256 + n * 8) = f32x2{st.a[0], st.b[0]};
            *(LAS f32x2*)(L + L_STX + w * 256 + 128 + n * 8) = f32x2{st.a[1], st.b[1]};
          }
        }
        if constexpr (i >= 48 && i < 72 && (i & 1) == 0) {
          constexpr int k = (i - 48) >> 1;
          const unsigned pc = (unsigned)((k & 3) * 4 + w) * 1024u;
          dma16(wsp + OFF_W1 + jo * 114688u + (k >> 2) * LU + pc, l16, lbase + W1U[1][k >> 2] * LU + pc);
        }
      });
      MFMA_FENCE();
#pragma unroll
      for (int t = 0; t < 6; ++t) { PINV(acc[t][0]); PINV(acc[t][1]); }
      f32x4 kva, kvb, za, zb;
      kv_epilogue(acc, L + L_STX + w * 256, (LAS float*)(L + L_CSQ), lane, kva, kvb, za, zb);
      kv_publish(L, L_KVX, rs, OFF_KVP + (unsigned)((16 + j) * 16 + g) * KVREC, lane, w, kva, kvb, za, zb);
      STAMP(SB + 14);
      publish(fb, FC_KV + (16 + j) * 2, tid);
      STAMP(SB + 15);
      dma_w123<64>(ws, OFF_WO + cs * 65536u, lbase + WOU[1] * LU, w, lane0);
      q_epilogue(acc, st, (LAS float*)(L + L_CSQ), lane, pq);
      if (b == 0 && tid == 0) __hip_atomic_store(fb + FO_EPOCH / 4, epoch, RLX_AGENT);
    }
  });
  STAMP_FLUSH(0, 64);
}

}

extern "C" void kernel_launch(void* const* d_in, const int* in_sizes, int n_in, void* d_out, int out_size, void* d_ws, size_t ws_size,
                              hipStream_t stream) {
  const u16* x = (const u16*)d_in[0];
  const u16* wqkv = (const u16*)d_in[1];
  const u16* wo = (const u16*)d_in[2];
  const u16* w1 = (const u16*)d_in[4];
  const u16* w2 = (const u16*)d_in[5];
  (void)hipFuncSetAttribute((const void*)kmain, hipFuncAttributeMaxDynamicSharedMemorySize, K2_LDS);
  kmain<<<256, 256, K2_LDS, stream>>>(x, wqkv, wo, w1, w2, (u16*)d_out, (unsigned char*)d_ws);
}
```

```cpp
#include <hip/hip_runtime.h>
#include <stdint.h>
#include <utility>

namespace {

typedef unsigned v4u __attribute__((ext_vector_type(4)));
typedef unsigned v2u __attribute__((ext_vector_type(2)));
typedef float f32x4 __attribute__((ext_vector_type(4)));
typedef float f32x2 __attribute__((ext_vector_type(2)));
typedef __bf16 bf2 __attribute__((ext_vector_type(2)));
typedef __bf16 bf8 __attribute__((ext_vector_type(8)));
typedef short s4v __attribute__((ext_vector_type(4)));
typedef unsigned short u16;
#define DI __device__ __forceinline__
#define GAS __attribute__((address_space(1)))
#define LAS __attribute__((address_space(3)))
typedef GAS unsigned gu32;
#define RLX_AGENT __ATOMIC_RELAXED, __HIP_MEMORY_SCOPE_AGENT
typedef __amdgpu_buffer_rsrc_t rsrc_t;

constexpr unsigned OFF_FLAGS = 0;
constexpr unsigned OFF_WQKV = 108544;
constexpr unsigned OFF_WO = OFF_WQKV + 1536u * 1024u;
constexpr unsigned OFF_W1 = OFF_WO + 512u * 1024u;
constexpr unsigned OFF_W2 = OFF_W1 + 1792u * 1024u;
constexpr unsigned OFF_CS1 = OFF_W2 + 1792u * 1024u;
constexpr unsigned OFF_CSQ = OFF_CS1 + 8u * 1792u * 4u;
constexpr unsigned OFF_PQ = OFF_CSQ + 16u * 96u * 4u;
constexpr unsigned KVREC = 2304;
constexpr unsigned OFF_KVP = OFF_PQ + 256u * 8192u;
constexpr unsigned OFF_ST = OFF_KVP + 2u * 256u * KVREC;
constexpr unsigned ST_BYTES = 128u * 1024u;
constexpr unsigned OFF_O = OFF_ST + 4u * ST_BYTES;
constexpr unsigned XF_BYTES = 128u * 16u * 1024u;
constexpr unsigned OFF_X1 = OFF_O + 2u * XF_BYTES;
constexpr unsigned OFF_X2 = OFF_X1 + 2u * XF_BYTES;
constexpr unsigned HF_BYTES = 128u * 56u * 1024u;
constexpr unsigned OFF_H = OFF_X2 + 2u * XF_BYTES;
constexpr unsigned WS_BYTES = OFF_H + 2u * HF_BYTES;
static_assert(OFF_O % 256 == 0 && OFF_KVP % 256 == 0 && OFF_PQ % 256 == 0 && OFF_ST % 256 == 0, "align");

constexpr unsigned FO_PRE = 0, FO_EPOCH = 1024, FO_TMO = 1088, FO_X0 = 2048  , FO_CNT = 10240;
constexpr int FC_O = 0;
constexpr int FC_X1 = 128;
constexpr int FC_H = 384;
constexpr int FC_X2 = 512;
constexpr int FC_KV = 640;
constexpr unsigned FLAG_BYTES = 10240 + 256 * 384;
static_assert(FO_CNT + (FC_KV + 64) * 128 <= FLAG_BYTES, "flags");
constexpr unsigned SPIN_CAP = 1u << 17;

constexpr int AUX_SC1 = 16;

constexpr unsigned LU = 16384;
constexpr unsigned L_SCR = 9 * LU;
constexpr unsigned L_CS1 = L_SCR;
constexpr unsigned L_CSQ = L_SCR + 512;
constexpr unsigned L_KVX = L_SCR + 1024;
constexpr unsigned L_CSP = L_SCR + 9728;
constexpr unsigned L_STX = L_SCR + 11264;
constexpr unsigned L_STAMP = L_SCR + 12288;
constexpr unsigned L_XB = 0;
constexpr unsigned W1UP = 0x3276810u;
constexpr int WOU = 2, QU = 2;
constexpr unsigned K2_LDS = 10 * LU;

DI f32x4 mfma(v4u a, v4u b, f32x4 c) {
  return __builtin_amdgcn_mfma_f32_16x16x32_bf16(__builtin_bit_cast(bf8, a), __builtin_bit_cast(bf8, b), c, 0, 0, 0);
}
template <bool FIRST> DI void mfma1(f32x4& c0, v4u a0, v4u b0) {
  if constexpr (FIRST) asm volatile("v_mfma_f32_16x16x32_bf16 %0, %1, %2, 0" : "=&v"(c0) : "v"(a0), "v"(b0));
  else asm volatile("v_mfma_f32_16x16x32_bf16 %0, %1, %2, %0" : "+v"(c0) : "v"(a0), "v"(b0));
}
template <bool FIRST> DI void mfma4a(f32x4 (&c)[4], v4u w0, v4u w1, v4u w2, v4u w3, v4u b) {
  if constexpr (FIRST)
    asm volatile("s_nop 1\n\tv_mfma_f32_16x16x32_bf16 %0, %4, %8, 0\n\tv_mfma_f32_16x16x32_bf16 %1, %5, %8, 0\n\t"
                 "v_mfma_f32_16x16x32_bf16 %2, %6, %8, 0\n\tv_mfma_f32_16x16x32_bf16 %3, %7, %8, 0"
                 : "=&v"(c[0]), "=&v"(c[1]), "=&v"(c[2]), "=&v"(c[3]) : "a"(w0), "a"(w1), "a"(w2), "a"(w3), "v"(b));
  else
    asm volatile("s_nop 1\n\tv_mfma_f32_16x16x32_bf16 %0, %4, %8, %0\n\tv_mfma_f32_16x16x32_bf16 %1, %5, %8, %1\n\t"
                 "v_mfma_f32_16x16x32_bf16 %2, %6, %8, %2\n\tv_mfma_f32_16x16x32_bf16 %3, %7, %8, %3"
                 : "+v"(c[0]), "+v"(c[1]), "+v"(c[2]), "+v"(c[3]) : "a"(w0), "a"(w1), "a"(w2), "a"(w3), "v"(b));
}
#define MFMA_FENCE() asm volatile("s_nop 15\n\ts_nop 3" ::: "memory")
DI void pinv(f32x4& x) { asm volatile("" : "+v"(x)); }
#define PINV(x) pinv(x)
DI unsigned pack2(float a, float b) {
  f32x2 v = {a, b};
  return __builtin_bit_cast(unsigned, __builtin_convertvector(v, bf2));
}
DI float bflo(unsigned u) { return __builtin_bit_cast(float, u << 16); }
DI float bfhi(unsigned u) { return __builtin_bit_cast(float, u & 0xffff0000u); }
DI float dot2(unsigned a, unsigned b, float c) {
  return __builtin_amdgcn_fdot2_f32_bf16(__builtin_bit_cast(bf2, a), __builtin_bit_cast(bf2, b), c, false);
}
constexpr float LOG2E = 1.4426950408889634f;
DI float phi(float t) { return __builtin_amdgcn_exp2f(fminf(t, 0.f) * LOG2E) + (fmaxf(t, 0.f) + 1e-6f); }
DI float silu(float t) { return t * __builtin_amdgcn_rcpf(1.0f + __builtin_amdgcn_exp2f(t * -LOG2E)); }
DI v4u ldb(rsrc_t rs, unsigned off) { return __builtin_amdgcn_raw_buffer_load_b128(rs, off, 0, 0); }
DI v4u ldb1(rsrc_t rs, unsigned off) { return __builtin_amdgcn_raw_buffer_load_b128(rs, off, 0, AUX_SC1); }
template <int IMM> DI v4u lda128(v4u srd, unsigned voff) {
  v4u t;
  asm volatile("buffer_load_dwordx4 %0, %1, %2, 0 offen offset:%3 sc1" : "=a"(t) : "v"(voff), "s"(srd), "i"(IMM) : "memory");
  return t;
}
DI void stb1(rsrc_t rs, unsigned off, v4u v) { __builtin_amdgcn_raw_buffer_store_b128(v, rs, off, 0, AUX_SC1); }
DI void stb1_64(rsrc_t rs, unsigned off, v2u v) { __builtin_amdgcn_raw_buffer_store_b64(v, rs, off, 0, AUX_SC1); }
#define DRAIN() asm volatile("s_waitcnt vmcnt(0)" ::: "memory")
#define LGKM0() asm volatile("s_waitcnt lgkmcnt(0)" ::: "memory")
#define BAR() do { asm volatile("s_waitcnt lgkmcnt(0)" ::: "memory"); __builtin_amdgcn_s_barrier(); asm volatile("" ::: "memory"); } while (0)
#define SCHED() __builtin_amdgcn_sched_barrier(0)
#define PIN(x) asm volatile("" : "+v"(x))
#define PIN4(a, o) asm volatile("" : "+v"(a[(o)]), "+v"(a[(o) + 1]), "+v"(a[(o) + 2]), "+v"(a[(o) + 3]))
#define STAMP(i) do {} while (0)
#define STAMP_INIT() do {} while (0)
#define STAMP_FLUSH(lo, hi) do {} while (0)

template <int... I, typename F> DI void static_for_impl(std::integer_sequence<int, I...>, F&& f) { (f(std::integral_constant<int, I>{}), ...); }
template <int N, typename F> DI void static_for(F&& f) { static_for_impl(std::make_integer_sequence<int, N>{}, f); }

DI void dma16(const void* sbase, unsigned voff, unsigned lds_dst) {
  unsigned keep;
  asm volatile("s_mov_b32 %0, m0\n\ts_mov_b32 m0, %3\n\ts_nop 3\n\tglobal_load_lds_dwordx4 %1, %2\n\ts_mov_b32 m0, %0"
               : "=&s"(keep) : "v"(voff), "s"(sbase), "s"(lds_dst) : "memory");
}
DI v4u tr_frag(LAS unsigned char* p) {
  s4v lo = __builtin_amdgcn_ds_read_tr16_b64_v4i16((LAS s4v*)(p));
  s4v hi = __builtin_amdgcn_ds_read_tr16_b64_v4i16((LAS s4v*)(p + 256));
  v2u l2 = __builtin_bit_cast(v2u, lo), h2 = __builtin_bit_cast(v2u, hi);
  return v4u{l2.x, l2.y, h2.x, h2.y};
}
DI float sum16(float v) {
  const unsigned u = __builtin_bit_cast(unsigned, v);
  unsigned u2 = u; PIN(u2);
  auto r = __builtin_amdgcn_permlane16_swap(u, u2, false, false);
  const unsigned a = r[0], b = r[1];
  return __builtin_bit_cast(float, a) + __builtin_bit_cast(float, b);
}
DI float sum32(float v) {
  const unsigned u = __builtin_bit_cast(unsigned, v);
  unsigned u2 = u; PIN(u2);
  auto r = __builtin_amdgcn_permlane32_swap(u, u2, false, false);
  const unsigned a = r[0], b = r[1];
  return __builtin_bit_cast(float, a) + __builtin_bit_cast(float, b);
}
DI float sum_q(float v) { return sum32(sum16(v)); }
DI v4u pair16(unsigned a0, unsigned a1, unsigned b0, unsigned b1) {
  auto r0 = __builtin_amdgcn_permlane16_swap(a0, b0, false, false);
  auto r1 = __builtin_amdgcn_permlane16_swap(a1, b1, false, false);
  const unsigned x0 = r0[0], x1 = r1[0], x2 = r0[1], x3 = r1[1];
  return v4u{x0, x1, x2, x3};
}

DI bool spin_fail(unsigned& sp, gu32* fb, unsigned epoch) {
  if (++sp > SPIN_CAP) { __hip_atomic_store(fb + FO_TMO / 4, epoch, RLX_AGENT); return true; }
  return (sp & 255u) == 0u && __hip_atomic_load(fb + FO_TMO / 4, RLX_AGENT) == epoch;
}
DI void poll_ge(gu32* fb, int line, unsigned want, unsigned epoch) {
  gu32* wd = fb + (FO_CNT / 4) + line * 32;
  for (unsigned sp = 0;;) {
    if ((unsigned)__builtin_amdgcn_readfirstlane(__hip_atomic_load(wd, RLX_AGENT)) >= want) return;
    __builtin_amdgcn_s_sleep(1);
    if (spin_fail(sp, fb, epoch)) return;
  }
}
DI void poll_ge2(gu32* fb, int line, unsigned want, unsigned epoch, int lane) {
  gu32* wd = fb + (FO_CNT / 4) + line * 32 + (lane & 1);
  for (unsigned sp = 0;;) {
    if (__all(__hip_atomic_load(wd, RLX_AGENT) >= want)) return;
    __builtin_amdgcn_s_sleep(1);
    if (spin_fail(sp, fb, epoch)) return;
  }
}
DI void arrive(gu32* fb, int line, int dw, int lane) { if (lane == 0) __hip_atomic_fetch_add(fb + (FO_CNT / 4) + line * 32 + dw, 1u, RLX_AGENT); }

struct Stats { float a[2], b[2]; };
DI void finish_stats(float s1, float s2, float& a, float& b) {
  const float mean = s1 * (1.0f / 512.0f);
  const float var = fmaxf(s2 * (1.0f / 512.0f) - mean * mean, 0.f);
  a = __builtin_amdgcn_rsqf(var + 1e-5f);
  b = -mean * a;
}

DI void publish(gu32* fb, int line, int tid) {
  DRAIN();
  BAR();
  arrive(fb, line, 0, tid);
}
DI void dma_rr(const unsigned char* src, unsigned ldst, int nkb, int w, int w0, int lane) {
  if (w < w0) return;
  const int nw = 4 - w0;
  const unsigned char* p = src + (unsigned)(w - w0) * 1024u;
  unsigned d = ldst + (unsigned)(w - w0) * 1024u;
#pragma unroll 1
  for (int i = w - w0; i < nkb; i += nw) {
    dma16(p, (unsigned)lane * 16u, d);
    p += nw * 1024; d += nw * 1024;
  }
}
template <bool TOR, int XO, bool FULL>
DI void tile2(f32x4& aA, f32x4& aB, const v4u (&xf)[32], v4u (&wf)[8], LAS unsigned char* cA, LAS unsigned char* cB, LAS unsigned char* nA, LAS unsigned char* nB) {
  constexpr int NS = FULL ? 16 : 8;
  static_for<NS>([&](auto S) {
    constexpr int s = S;
    const v4u fa = wf[(2 * s) & 7], fb = wf[(2 * s + 1) & 7];
    if constexpr (s + 4 < NS) { wf[(2 * s) & 7] = *(LAS v4u*)(cA + (s + 4) * 1024); wf[(2 * s + 1) & 7] = *(LAS v4u*)(cB + (s + 4) * 1024); }
    else { wf[(2 * s) & 7] = *(LAS v4u*)(nA + (s + 4 - NS) * 1024); wf[(2 * s + 1) & 7] = *(LAS v4u*)(nB + (s + 4 - NS) * 1024); }
    const v4u xa = xf[XO + s], xb = xf[XO + (FULL ? 0 : 8) + s];
    if constexpr (TOR) { mfma1<s == 0>(aA, fa, xa); mfma1<s == 0>(aB, fb, xb); }
    else { mfma1<s == 0>(aA, xa, fa); mfma1<s == 0>(aB, xb, fb); }
    SCHED();
  });
  MFMA_FENCE();
  PINV(aA); PINV(aB);
}
DI void ring_fill(v4u (&wf)[8], LAS unsigned char* a, LAS unsigned char* b) {
#pragma unroll
  for (int i = 0; i < 4; ++i) { wf[2 * i] = *(LAS v4u*)(a + i * 1024); wf[2 * i + 1] = *(LAS v4u*)(b + i * 1024); }
}
DI void res_update(v2u (&res)[4], const f32x4 (&acc)[4]) {
#pragma unroll
  for (int c = 0; c < 4; ++c) {
    const unsigned t0 = pack2(acc[c][0], acc[c][1]), t1 = pack2(acc[c][2], acc[c][3]);
    res[c].x = pack2(bflo(res[c].x) + bflo(t0), bfhi(res[c].x) + bfhi(t0));
    res[c].y = pack2(bflo(res[c].y) + bflo(t1), bfhi(res[c].y) + bfhi(t1));
  }
}
DI void store_x_tile(rsrc_t rs, unsigned xoff, unsigned stoff, const v2u (&res)[4], int myrt, int cs, int lane) {
  const int q = lane >> 4, n = lane & 15;
  const unsigned kg = (q & 1) * 2 + (q >> 1);
#pragma unroll
  for (int c2 = 0; c2 < 2; ++c2) {
    const v4u o = pair16(res[2 * c2].x, res[2 * c2].y, res[2 * c2 + 1].x, res[2 * c2 + 1].y);
    stb1(rs, xoff + (unsigned)(myrt * 16 + 2 * cs + c2) * 1024u + (kg * 16 + n) * 16, o);
  }
  float s1 = 0, s2 = 0;
#pragma unroll
  for (int c = 0; c < 4; ++c) {
    s1 = dot2(res[c].x, 0x3f803f80u, s1); s2 = dot2(res[c].x, res[c].x, s2);
    s1 = dot2(res[c].y, 0x3f803f80u, s1); s2 = dot2(res[c].y, res[c].y, s2);
  }
  s1 = sum_q(s1); s2 = sum_q(s2);
  if (q == 0) stb1_64(rs, stoff + (unsigned)myrt * 1024u + n * 64 + cs * 8, __builtin_bit_cast(v2u, f32x2{s1, s2}));
}
DI void stats_from(v4u sp, float& a, float& b) {
  const f32x4 p = __builtin_bit_cast(f32x4, sp);
  finish_stats(sum_q(p[0] + p[2]), sum_q(p[1] + p[3]), a, b);
}

__global__ void __launch_bounds__(256, 1) kmain(const u16* __restrict__ x, const u16* __restrict__ wqkv, const u16* __restrict__ wo,
                                                const u16* __restrict__ w1, const u16* __restrict__ w2g, u16* __restrict__ out,
                                                unsigned char* __restrict__ ws) {
  extern __shared__ __attribute__((aligned(16))) unsigned char smem[];
  LAS unsigned char* L = (LAS unsigned char*)smem;
  constexpr unsigned LSTAMP = L_STAMP;
  const unsigned lbase = (unsigned)(size_t)L;
  const int tid = threadIdx.x, lane0 = tid & 63, w = __builtin_amdgcn_readfirstlane(tid >> 6);
  const int b = blockIdx.x;
  const rsrc_t rs = __builtin_amdgcn_make_buffer_rsrc((void*)ws, 0, WS_BYTES, 0x00020000);
  const v4u srd = {(unsigned)(size_t)ws, (unsigned)((size_t)ws >> 32) & 0xffffu, WS_BYTES, 0x00020000u};
  gu32* fb = (gu32*)(ws + OFF_FLAGS);
  const f32x4 z4 = {0, 0, 0, 0};
  const v4u ones = {0x3f803f80u, 0x3f803f80u, 0x3f803f80u, 0x3f803f80u};
  const int g = 2 * (b & 7) + ((b >> 3) >> 4), j = (b >> 3) & 15;
  const int g0 = 4 * ((b & 7) >> 1) + (b >> 6), j0 = 8 * (b & 1) + ((b >> 3) & 7);
  const int hh = j >> 3, cs = j & 7;
  const int myrt = 8 * g + 4 * hh + w;
  const int rt0 = 8 * g + 2 * w;
#define FRESH() int lane = lane0; PIN(lane); const int tid = w * 64 + lane; (void)tid; const int q = lane >> 4, n = lane & 15; const unsigned l16 = (unsigned)lane * 16u; LAS unsigned char* Ll = L + l16; (void)q; (void)n; (void)Ll;
  using std::integral_constant;
#define IC(x) integral_constant<int, (x)>{}
#define VMW(n) asm volatile("s_waitcnt vmcnt(%0)" :: "i"(n) : "memory")
  STAMP_INIT();
  STAMP(0);
  const unsigned epoch = __hip_atomic_load(fb + FO_EPOCH / 4, RLX_AGENT) + 1u;

  v4u w2[14][4];
  auto ldw2 = [&](auto LO, auto N, unsigned l16) {
    asm volatile("s_nop 4" ::: "memory");
    static_for<N>([&](auto I) {
      constexpr int i = LO + I;
      w2[i >> 2][i & 3] = lda128<(i & 3) * 1024>(srd, l16 + OFF_W2 + (unsigned)((cs * 4 + w) * 56 + (i & ~3)) * 1024u);
    });
  };
  v2u res[4];

  {
    FRESH();
    const int drow = lane >> 2, dch = (lane & 3) ^ (((drow >> 3) & 1) << 1);
    if (w < 2) {
      v2u r0[4], r1[4];
      const GAS v2u* xp = (const GAS v2u*)(x + (size_t)(16 * myrt + n) * 512 + 64 * cs + 4 * q);
#pragma unroll
      for (int c = 0; c < 4; ++c) { r0[c] = xp[c * 4]; r1[c] = xp[c * 4 + 32 * 128]; }
      store_x_tile(rs, OFF_X2, OFF_ST + 2 * ST_BYTES, r0, myrt, cs, lane);
      store_x_tile(rs, OFF_X2, OFF_ST + 2 * ST_BYTES, r1, myrt + 2, cs, lane);
#pragma unroll
      for (int c = 0; c < 4; ++c) res[c] = r0[c];
      DRAIN();
      if (lane < 2) __hip_atomic_store(fb + (FO_X0 + (unsigned)(myrt + 2 * lane) * 64u) / 4 + cs, epoch, RLX_AGENT);
    } else {
      const unsigned voff = (unsigned)(drow * 1536 + 32 * j0 + dch * 8) * 2u;
#pragma unroll 1
      for (int it = 0; it < 48; ++it) {
        const int seg = it % 3, rb = 2 * (it / 3) + (w & 1);
        const int t = seg == 0 ? 4 + (w & 1) : seg - 1 + 2 * (w & 1);
        dma16((const unsigned char*)wqkv + (size_t)(rb * 16 * 1536 + seg * 512) * 2, voff, lbase + (QU + t) * LU + (rb >> 1) * 1024);
      }
      const GAS v2u* xp = (const GAS v2u*)(x + (size_t)(16 * myrt + n) * 512 + 64 * cs + 4 * q);
#pragma unroll
      for (int c = 0; c < 4; ++c) res[c] = xp[c * 4];
    }
    const u16* psrc; int pld, pp, pks, pkind;
    if (b < 112) { psrc = w1; pld = 1792; pp = b >> 1; pks = 8 * (b & 1) + 2 * w; pkind = 0; }
    else {
      const int u = (b - 112) * 8 + 2 * w;
      if (u < 896) { psrc = w2g; pld = 512; pp = u / 56; pks = u % 56; pkind = 1; }
      else { psrc = wo; pld = 512; pp = (u - 896) >> 4; pks = (u - 896) & 15; pkind = 2; }
    }
    v4u pt[4];
#pragma unroll
    for (int i = 0; i < 4; ++i) {
      const int ks = pks + (i >> 1);
      const int prow = pkind == 1 ? 112 * (2 * (ks / 7) + (i & 1)) + 16 * (ks % 7) : 32 * ks + 16 * (i & 1);
      pt[i] = *(const GAS v4u*)(psrc + (size_t)(prow + drow) * pld + 32 * pp + dch * 8);
    }
    SCHED();
    if (tid < 24) stb1(rs, OFF_FLAGS + FO_CNT + b * 384 + tid * 16, v4u{0u, 0u, 0u, 0u});
    STAMP(1);
    LAS unsigned char* B = L + L_XB + w * 8192;
    STAMP(2);
    const int r4 = n >> 2, c4 = n & 3;
    unsigned trb[2];
#pragma unroll
    for (int ct = 0; ct < 2; ++ct) trb[ct] = (unsigned)(((q & 1) * 8 + r4) * 64 + (2 * (ct ^ (q & 1)) + (c4 >> 1)) * 16 + (c4 & 1) * 8);
    auto trf = [&](LAS unsigned char* blk0, unsigned bstr, int ct) -> v4u { return tr_frag(blk0 + (q >> 1) * bstr + trb[ct]); };
    {
#pragma unroll
      for (int i = 0; i < 4; ++i) *(LAS v4u*)(B + i * 1024 + l16) = pt[i];
      LGKM0();
      f32x4 c1[2] = {z4, z4};
#pragma unroll
      for (int u = 0; u < 2; ++u)
#pragma unroll
        for (int ct = 0; ct < 2; ++ct) {
          const v4u f = trf(B + u * 2048, 1024, ct);
          const int gct = 2 * pp + ct, ks = pks + u;
          unsigned dst;
          if (pkind == 0) dst = OFF_W1 + (unsigned)((gct / 7) * 112 + (gct % 7) * 16 + ks) * 1024u;
          else if (pkind == 1) { const int p = ks / 7, t = ks % 7; dst = OFF_W2 + (unsigned)(((gct >> 2) * 4 + (p >> 1)) * 56 + (2 * t + (p & 1)) * 4 + (gct & 3)) * 1024u; }
          else dst = OFF_WO + (unsigned)((gct >> 2) * 64 + (gct & 3) * 16 + ks) * 1024u;
          stb1(rs, dst + l16, f);
          c1[ct] = mfma(f, ones, c1[ct]);
        }
      if (pkind == 0 && n == 0) {
        const int part = (b & 1) * 4 + w;
#pragma unroll
        for (int ct = 0; ct < 2; ++ct)
          stb1(rs, OFF_CS1 + (unsigned)(part * 1792 + 32 * pp + 16 * ct + 4 * q) * 4u, __builtin_bit_cast(v4u, c1[ct]));
      }
    }
    STAMP(3);
    DRAIN();
    BAR();
    STAMP(4);
    {
      f32x4 csum[6] = {z4, z4, z4, z4, z4, z4};
#pragma unroll 1
      for (int i = 0; i < 4; ++i) {
        const int ks = 4 * w + i;
#pragma unroll
        for (int seg = 0; seg < 3; ++seg) {
          const int t0 = seg == 0 ? 4 : seg - 1, t1 = seg == 0 ? 5 : seg + 1;
          LAS unsigned char* blk = L + (QU + t0) * LU + ks * 1024;
          const v4u f0 = trf(blk, (t1 - t0) * LU, 0), f1 = trf(blk, (t1 - t0) * LU, 1);
          if (seg == 0) { csum[4] = mfma(f0, ones, csum[4]); csum[5] = mfma(f1, ones, csum[5]); }
          else { csum[t0] = mfma(ones, f0, csum[t0]); csum[t1] = mfma(ones, f1, csum[t1]); }
          *(LAS v4u*)(blk + l16) = f0;
          *(LAS v4u*)(blk + (t1 - t0) * LU + l16) = f1;
          if (ks == g0) {
            stb1(rs, OFF_WQKV + (unsigned)(j0 * 96 + t0 * 16 + g0) * 1024u + l16, f0);
            stb1(rs, OFF_WQKV + (unsigned)(j0 * 96 + t1 * 16 + g0) * 1024u + l16, f1);
          }
        }
      }
      LAS float* csw = (LAS float*)(L + L_CSP) + w * 96;
#pragma unroll
      for (int t = 4; t < 6; ++t) if (n == 0) *(LAS f32x4*)(csw + t * 16 + 4 * q) = csum[t];
#pragma unroll
      for (int t = 0; t < 4; ++t) if (q == 0) csw[t * 16 + n] = csum[t][0];
    }
    BAR();
    if (tid < 96) {
      LAS float* c = (LAS float*)(L + L_CSP);
      const float s = (c[tid] + c[96 + tid]) + (c[192 + tid] + c[288 + tid]);
      ((LAS float*)(L + L_CSQ))[tid] = s;
      if (g0 == 0) __hip_atomic_store((GAS float*)(ws + OFF_CSQ) + j0 * 96 + tid, s, RLX_AGENT);
    }
    DRAIN();
    BAR();
    if (tid == 0) __hip_atomic_store(fb + FO_PRE / 4 + b, epoch, RLX_AGENT);
    STAMP(5);
  }

#pragma unroll 1
  for (int l = 0; l < 2; ++l) {
    const int SB = 10 + l * 24;
    (void)SB;
    {
      FRESH();
      v4u xf[32];
      const int gq = l ? g : g0, jq = l ? j : j0, rq = 8 * gq + 2 * w;
      const unsigned ab = OFF_X2 + l * XF_BYTES + (unsigned)(rq * 16) * 1024u + l16;
      const unsigned sto = OFF_ST + (2 + l) * ST_BYTES + (unsigned)rq * 1024u + n * 64 + q * 16;
      auto wait_x = [&](int H) {
        if (l) poll_ge(fb, FC_X2 + g * 8 + 2 * w + H, 8u, epoch);
        else {
          gu32* wd = fb + (FO_X0 + (unsigned)(rq + H) * 64u) / 4 + (lane & 7);
          for (unsigned sp2 = 0;;) {
            if (__all(__hip_atomic_load(wd, RLX_AGENT) == epoch)) break;
            __builtin_amdgcn_s_sleep(1);
            if (spin_fail(sp2, fb, epoch)) break;
          }
        }
      };
      wait_x(0);
      v4u sp = ldb1(rs, sto);
#pragma unroll
      for (int f = 0; f < 16; ++f) xf[f] = ldb1(rs, ab + f * 1024);
      STAMP(SB + 13);
      LAS unsigned char* stx = L + L_STX + w * 256;
      LAS unsigned char* kvx = L + L_KVX + w * 2176;
      const unsigned pqo = OFF_PQ + (unsigned)(((gq * 16 + jq) * 4 + w) * 2) * 1024u + l16;
      v4u wf[8];
      ring_fill(wf, Ll + QU * LU, Ll + (QU + 1) * LU);
      float sa1, sb1;
      f32x4 ab0, ab1;
      auto mkstats = [&]() {
        stats_from(sp, sa1, sb1);
        if (q == 0) *(LAS f32x2*)(stx + n * 8) = f32x2{sa1, sb1};
        ab0 = *(LAS f32x4*)(stx + q * 32); ab1 = *(LAS f32x4*)(stx + q * 32 + 16);
      };
      auto head = [&](auto HH, int h) {
        constexpr int H = HH;
        LAS unsigned char* pk_ = Ll + (QU + 2 * h) * LU;
        LAS unsigned char* pv_ = pk_ + LU;
        LAS unsigned char* pq_ = Ll + (QU + 4 + h) * LU;
        LAS unsigned char* pn_ = Ll + (QU + 2 * (h ^ 1)) * LU;
        f32x4 ak, av, aq, aq2;
        tile2<false, H * 16, true>(ak, av, xf, wf, pk_, pv_, pq_, pq_ + 8192);
        const float ck = ((LAS float*)(L + L_CSQ))[(2 * h) * 16 + n], cv = ((LAS float*)(L + L_CSQ))[(2 * h + 1) * 16 + n];
        const f32x4 cq = *(LAS f32x4*)(L + L_CSQ + ((4 + h) * 16 + 4 * q) * 4);
        float a[4], bb[4];
#pragma unroll
        for (int r = 0; r < 4; ++r) {
          const float sa = r < 2 ? ab0[r * 2] : ab1[(r - 2) * 2], sb = r < 2 ? ab0[r * 2 + 1] : ab1[(r - 2) * 2 + 1];
          a[r] = phi(fmaf(ak[r], sa, sb * ck));
          bb[r] = fmaf(av[r], sa, sb * cv);
        }
        const v4u PK = {pack2(a[0], a[1]), pack2(a[2], a[3]), 0u, 0u}, V = {pack2(bb[0], bb[1]), pack2(bb[2], bb[3]), 0u, 0u};
        f32x4 kv = mfma(PK, V, z4), zz = mfma(PK, ones, z4);
        if constexpr (H == 1) { kv += *(LAS f32x4*)(kvx + h * 1024 + l16); zz += *(LAS f32x4*)(kvx + 2048 + h * 64 + q * 16); }
        *(LAS f32x4*)(kvx + h * 1024 + l16) = kv;
        LGKM0();
        if (n == 0) *(LAS f32x4*)(kvx + 2048 + h * 64 + q * 16) = zz;
        tile2<true, H * 16, false>(aq, aq2, xf, wf, pq_, pq_ + 8192, pn_, pn_ + LU);
        float v[4];
#pragma unroll
        for (int r = 0; r < 4; ++r) v[r] = phi(fmaf(aq[r] + aq2[r], sa1, sb1 * cq[r]));
        stb1_64(rs, pqo + H * 1024 + h * 8, v2u{pack2(v[0], v[1]), pack2(v[2], v[3])});
      };
      mkstats();
      head(IC(0), 0);
      wait_x(1);
      sp = ldb1(rs, sto + 1024);
#pragma unroll
      for (int f = 16; f < 32; ++f) xf[f] = ldb1(rs, ab + f * 1024);
      head(IC(0), 1);
      mkstats();
#pragma unroll 1
      for (int h = 0; h < 2; ++h) {
        head(IC(1), h);
        if (l) {
#pragma unroll 1
          for (int k = 6 * h; k < 6 * h + 6; ++k) {
            const unsigned pc = (unsigned)((k & 3) * 4 + w) * 1024u;
            dma16(ws + OFF_W1 + j * 114688u + (k >> 2) * LU + pc, l16, lbase + ((W1UP >> (4 * (k >> 2))) & 15u) * LU + pc);
          }
        }
      }
      STAMP(SB + 19);
      if (w == 0 && l == 0) {
        for (unsigned sp2 = 0;;) {
          const v4u v = ldb1(rs, OFF_FLAGS + FO_PRE + l16);
          if (__all(v.x == epoch && v.y == epoch && v.z == epoch && v.w == epoch)) break;
          __builtin_amdgcn_s_sleep(4);
          if (spin_fail(sp2, fb, epoch)) break;
        }
      }
      BAR();
      if (w < 2 || (w == 2 && lane < 8)) {
        const unsigned o = w * 1024 + l16;
        f32x4 s = *(LAS f32x4*)(L + L_KVX + o);
#pragma unroll
        for (int ww = 1; ww < 4; ++ww) s += *(LAS f32x4*)(L + L_KVX + ww * 2176 + o);
        stb1(rs, OFF_KVP + (unsigned)((l * 16 + jq) * 16 + gq) * KVREC + o, __builtin_bit_cast(v4u, s));
      }
      STAMP(SB + 14);
      publish(fb, FC_KV + (l * 16 + jq) * 2, tid);
      STAMP(SB + 15);
      dma_rr(ws + OFF_WO + cs * 65536u, lbase + WOU * LU, 64, w, 1, lane);
      if (l == 0) {
        if (b == 0 && tid == 0) __hip_atomic_store(fb + FO_EPOCH / 4, epoch, RLX_AGENT);
        dma_rr(ws + OFF_W1 + j * 114688u, lbase, 32, w, 1, lane);
        if (tid < 112) {
          float csv = 0.f;
#pragma unroll
          for (int i = 0; i < 8; ++i) csv += __hip_atomic_load((GAS float*)(ws + OFF_CS1) + i * 1792 + 112 * j + tid, RLX_AGENT);
          ((LAS float*)(L + L_CS1))[tid] = csv;
        } else if (tid >= 128 && tid < 224) {
          ((LAS float*)(L + L_CSQ))[tid - 128] = __hip_atomic_load((GAS float*)(ws + OFF_CSQ) + j * 96 + tid - 128, RLX_AGENT);
        }
      }
      ldw2(IC(0), IC(14), l16);
    }
    if (w == 0) poll_ge(fb, FC_KV + (l * 16 + j) * 2, 16u, epoch);
    BAR();
    STAMP(SB + 0);
    {
      FRESH();
      v4u pq[2];
#pragma unroll
      for (int rt = 0; rt < 2; ++rt) pq[rt] = ldb1(rs, OFF_PQ + (unsigned)(((g * 16 + j) * 4 + w) * 2 + rt) * 1024u + l16);
      const unsigned rb = OFF_KVP + (unsigned)((l * 16 + j) * 16) * KVREC;
      if (w < 3) {
        const unsigned o = rb + (w < 2 ? (unsigned)w * 1024u + l16 : 2048u + (unsigned)(lane & 7) * 16u);
        v4u a[16];
#pragma unroll
        for (int i = 0; i < 16; ++i) a[i] = ldb1(rs, o + i * KVREC);
        SCHED();
        f32x4 s[4];
#pragma unroll
        for (int i = 0; i < 4; ++i)
          s[i] = (__builtin_bit_cast(f32x4, a[4 * i]) + __builtin_bit_cast(f32x4, a[4 * i + 1])) + (__builtin_bit_cast(f32x4, a[4 * i + 2]) + __builtin_bit_cast(f32x4, a[4 * i + 3]));
        const f32x4 t = (s[0] + s[1]) + (s[2] + s[3]);
        const v2u pk = v2u{pack2(t[0], t[1]), pack2(t[2], t[3])};
        LAS unsigned char* kx = L + L_KVX + 4 * 2176 - 1088;
        (void)kx;
        if (w < 2) *(LAS v2u*)(L + L_KVX + w * 512 + lane * 8) = pk;
        else if (lane < 8) *(LAS v2u*)(L + L_KVX + 1024 + lane * 8) = pk;
      }
      BAR();
      const v2u ka = *(LAS v2u*)(L + L_KVX + lane * 8), kb = *(LAS v2u*)(L + L_KVX + 512 + lane * 8);
      const v2u zz = *(LAS v2u*)(L + L_KVX + 1024 + ((n & 1) * 4 + q) * 8);
      const v4u Aa = {ka.x, ka.y, 0u, 0u};
      const v4u Ab = {0u, 0u, kb.x, kb.y};
      const bool za_ = (n & 3) == 0, zb_ = (n & 3) == 1;
      const v4u Az = {za_ ? zz.x : 0u, za_ ? zz.y : 0u, zb_ ? zz.x : 0u, zb_ ? zz.y : 0u};
#pragma unroll
      for (int rt = 0; rt < 2; ++rt) {
        const f32x4 na = mfma(Aa, pq[rt], z4), nb = mfma(Ab, pq[rt], z4), dn = mfma(Az, pq[rt], z4);
        const float ra = __builtin_amdgcn_rcpf(dn[0] + 1e-6f), rb2 = __builtin_amdgcn_rcpf(dn[1] + 1e-6f);
        const v4u o = pair16(pack2(na[0] * ra, na[1] * ra), pack2(na[2] * ra, na[3] * ra), pack2(nb[0] * rb2, nb[1] * rb2), pack2(nb[2] * rb2, nb[3] * rb2));
        const unsigned kg = (q & 1) * 2 + (q >> 1);
        stb1(rs, OFF_O + l * XF_BYTES + (unsigned)((rt0 + rt) * 16 + j) * 1024u + (kg * 16 + n) * 16, o);
      }
      STAMP(SB + 1);
      DRAIN();
      arrive(fb, FC_O + (l * 16 + g) * 4 + w, 0, lane);
      BAR();
      STAMP(SB + 2);
#pragma unroll 1
      for (int k = l ? 4 : 0; k < 12; ++k) {
        const unsigned t = 2 + (k >> 2), pc = (unsigned)((k & 3) * 4 + w) * 1024u;
        dma16(ws + OFF_W1 + j * 114688u + t * LU + pc, l16, lbase + ((W1UP >> (4 * t)) & 15u) * LU + pc);
      }
    }
    poll_ge(fb, FC_O + (l * 16 + g) * 4 + ((4 * hh + w) >> 1), 16u, epoch);
    STAMP(SB + 3);
    {
      FRESH();
      const unsigned ab = OFF_O + l * XF_BYTES + (unsigned)(myrt * 16) * 1024u + l16;
      v4u xf[32];
#pragma unroll
      for (int ks = 0; ks < 16; ++ks) xf[ks] = ldb1(rs, ab + ks * 1024);
      f32x4 acc[4];
      v4u wf[8];
      ring_fill(wf, Ll + WOU * LU, Ll + (WOU + 1) * LU);
      tile2<true, 0, true>(acc[0], acc[1], xf, wf, Ll + WOU * LU, Ll + (WOU + 1) * LU, Ll + (WOU + 2) * LU, Ll + (WOU + 3) * LU);
      tile2<true, 0, true>(acc[2], acc[3], xf, wf, Ll + (WOU + 2) * LU, Ll + (WOU + 3) * LU, Ll + WOU * LU, Ll + (WOU + 1) * LU);
      res_update(res, acc);
      store_x_tile(rs, OFF_X1 + l * XF_BYTES, OFF_ST + l * ST_BYTES, res, myrt, cs, lane);
      STAMP(SB + 4);
      DRAIN();
      arrive(fb, FC_X1 + (l * 16 + g) * 8 + 4 * hh + w, 0, lane);
      BAR();
      STAMP(SB + 5);
#pragma unroll 1
      for (int k = 0; k < 8; ++k) {
        const unsigned t = 5 + (k >> 2), pc = (unsigned)((k & 3) * 4 + w) * 1024u;
        dma16(ws + OFF_W1 + j * 114688u + t * LU + pc, l16, lbase + ((W1UP >> (4 * t)) & 15u) * LU + pc);
      }
      ldw2(IC(14), IC(14), l16);
    }

    poll_ge(fb, FC_X1 + (l * 16 + g) * 8 + 2 * w, 8u, epoch);
    poll_ge(fb, FC_X1 + (l * 16 + g) * 8 + 2 * w + 1, 8u, epoch);
    STAMP(SB + 6);
    {
      FRESH();
      const int hline_out = FC_H + ((l * 16 + g) * 2 + (w >> 1)) * 2, hline_in = FC_H + ((l * 16 + g) * 2 + hh) * 2;
      const unsigned ab = OFF_X1 + l * XF_BYTES + (unsigned)(rt0 * 16) * 1024u + l16;
      const unsigned sto = OFF_ST + l * ST_BYTES + (unsigned)rt0 * 1024u + n * 64 + q * 16;
      const v4u sp0 = ldb1(rs, sto), sp1 = ldb1(rs, sto + 1024);
      v4u xf[32];
#pragma unroll
      for (int f = 0; f < 32; ++f) xf[f] = ldb1(rs, ab + f * 1024);
      VMW(34);
      BAR();
      const unsigned hst = OFF_H + l * HF_BYTES + ((unsigned)rt0 * 56u + (unsigned)(j >> 1) * 7u) * 1024u + ((2 * (j & 1) + (q >> 1)) * 16 + n) * 16;
      v4u wf[8];
      ring_fill(wf, Ll, Ll + LU);
      auto half1 = [&](auto HH) {
        constexpr int H = HH;
        float sa, sb;
        stats_from(H ? sp1 : sp0, sa, sb);
        sa *= LOG2E; sb *= LOG2E;
        auto act = [&](const f32x4& acc, int t, unsigned& y0, unsigned& y1) {
          const f32x4 c = *(LAS f32x4*)(L + L_CS1 + (t * 16 + 4 * q) * 4);
          float y[4];
#pragma unroll
          for (int r = 0; r < 4; ++r) {
            const float v = fmaf(acc[r], sa, sb * c[r]);
            y[r] = v * __builtin_amdgcn_rcpf(fmaf(__builtin_amdgcn_exp2f(-v), LOG2E, LOG2E));
          }
          y0 = pack2(y[0], y[1]); y1 = pack2(y[2], y[3]);
        };
#pragma unroll 1
        for (int p = 0; p < 3; ++p) {
          LAS unsigned char* u0 = Ll + ((W1UP >> (8 * p)) & 15u) * LU;
          LAS unsigned char* u1 = Ll + ((W1UP >> (8 * p + 4)) & 15u) * LU;
          LAS unsigned char* u2 = Ll + ((W1UP >> (8 * p + 8)) & 15u) * LU;
          LAS unsigned char* u3 = p < 2 ? Ll + ((W1UP >> (8 * p + 12)) & 15u) * LU : u2 + 8192;
          f32x4 a0, a1;
          tile2<true, H * 16, true>(a0, a1, xf, wf, u0, u1, u2, u3);
          unsigned y00, y01, y10, y11;
          act(a0, 2 * p, y00, y01); act(a1, 2 * p + 1, y10, y11);
          stb1(rs, hst + H * 56 * 1024 + (2 * p + (q & 1)) * 1024, pair16(y00, y01, y10, y11));
          if constexpr (H == 1) { if (p == 1) { VMW(2); arrive(fb, hline_out + 0, w & 1, lane); } }
        }
        f32x4 a6, a7;
        LAS unsigned char* u6 = Ll + ((W1UP >> 24) & 15u) * LU;
        tile2<true, H * 16, false>(a6, a7, xf, wf, u6, u6 + 8192, Ll, Ll + LU);
        a6 += a7;
        unsigned y60, y61;
        act(a6, 6, y60, y61);
        return v2u{y60, y61};
      };
      { const v2u o6 = half1(IC(0)); stb1_64(rs, hst + 6 * 1024 + (q & 1) * 8, o6); }
      STAMP(SB + 17);
      const v2u o61 = half1(IC(1));
      STAMP(SB + 18);
      constexpr int WIN = 16;
      v4u a[WIN];
      auto hoff = [](int f) -> unsigned { return (unsigned)(((f >> 1) & 1) * 7 + (f >> 2)) * 1024u + (unsigned)(f & 1) * (2 * 56 * 1024); };
      const unsigned hb0 = OFF_H + l * HF_BYTES + (unsigned)((8 * g + 4 * hh) * 56 + 14 * w) * 1024u + l16;
      poll_ge2(fb, hline_in, 16u, epoch, lane);
#pragma unroll
      for (int f = 0; f < WIN; ++f) a[f] = ldb1(rs, hb0 + hoff(f));
      stb1_64(rs, hst + 56 * 1024 + 6 * 1024 + (q & 1) * 8, o61);
      ldw2(IC(28), IC(28), l16);
      STAMP(SB + 7);
      BAR();
#pragma unroll 1
      for (int H = 0; H < 2; ++H) {
        const unsigned hb = hb0 + (unsigned)H * (56 * 1024);
        f32x4 acc2[2][4];
        static_for<28>([&](auto F) {
          constexpr int f = F, wi = 2 * (f >> 2) + ((f >> 1) & 1);
          const v4u cur = a[f % WIN];
          if constexpr (f == 12) {
            VMW(12);
            if (H == 0) arrive(fb, hline_out + 1, w & 1, lane);
            else if (l == 0 && (w & 1) == 0) arrive(fb, FC_X2 + g * 8 + 4 * hh + w, 0, lane);
          }
          if constexpr (f + WIN < 28) a[f % WIN] = ldb1(rs, hb + hoff(f + WIN));
          mfma4a<(f < 2)>(acc2[f & 1], w2[wi][0], w2[wi][1], w2[wi][2], w2[wi][3], cur);
          if constexpr (f >= 16) {
            if (l == 0) {
              const unsigned o = (unsigned)(H * 48 + (f - 16) * 4 + w) * 1024u;
              dma16(ws + OFF_WQKV + j * 98304u + o, l16, lbase + QU * LU + o);
            }
          }
          SCHED();
        });
        MFMA_FENCE();
#pragma unroll
        for (int i2 = 0; i2 < 2; ++i2)
#pragma unroll
          for (int c = 0; c < 4; ++c) PINV(acc2[i2][c]);
        STAMP(SB + 9 + H);
        if (H == 0) {
          poll_ge2(fb, hline_in + 1, 16u, epoch, lane);
#pragma unroll
          for (int f = 0; f < WIN; ++f) a[f] = ldb1(rs, hb + 56 * 1024 + hoff(f));
        } else {
          BAR();
        }
#pragma unroll
        for (int i2 = 0; i2 < 2; ++i2) {
          const int own = H + 2 * i2;
          if (w != own) {
            const int slot = w - (w > own ? 1 : 0);
#pragma unroll
            for (int c = 0; c < 4; ++c) *(LAS f32x4*)(Ll + i2 * LU + (slot * 4 + c) * 1024) = acc2[i2][c];
          }
        }
        BAR();
        if ((w & 1) == H) {
          f32x4 s[4];
#pragma unroll
          for (int c = 0; c < 4; ++c) s[c] = (w >> 1) ? acc2[1][c] : acc2[0][c];
#pragma unroll
          for (int sl = 0; sl < 3; ++sl)
#pragma unroll
            for (int c = 0; c < 4; ++c) s[c] += *(LAS f32x4*)(Ll + (w >> 1) * LU + (sl * 4 + c) * 1024);
          res_update(res, s);
          if (l == 0) {
            store_x_tile(rs, OFF_X2 + XF_BYTES, OFF_ST + 3 * ST_BYTES, res, myrt, cs, lane);
          } else {
#pragma unroll
            for (int c2 = 0; c2 < 2; ++c2) {
              const v4u o = pair16(res[2 * c2].x, res[2 * c2].y, res[2 * c2 + 1].x, res[2 * c2 + 1].y);
              *(GAS v4u*)(out + (size_t)(16 * myrt + n) * 512 + 64 * cs + 16 * (2 * c2 + (q & 1)) + 8 * (q >> 1)) = o;
            }
          }
        }
      }
      STAMP(SB + 11);
      if (l == 0) {
        DRAIN();
        if ((w & 1) == 1) arrive(fb, FC_X2 + g * 8 + 4 * hh + w, 0, lane);
        BAR();
      }
    }
  }
  STAMP_FLUSH(0, 64);
}

}

extern "C" void kernel_launch(void* const* d_in, const int* in_sizes, int n_in, void* d_out, int out_size, void* d_ws, size_t ws_size,
                              hipStream_t stream) {
  const u16* x = (const u16*)d_in[0];
  const u16* wqkv = (const u16*)d_in[1];
  const u16* wo = (const u16*)d_in[2];
  const u16* w1 = (const u16*)d_in[4];
  const u16* w2 = (const u16*)d_in[5];
  (void)hipFuncSetAttribute((const void*)kmain, hipFuncAttributeMaxDynamicSharedMemorySize, K2_LDS);
  kmain<<<256, 256, K2_LDS, stream>>>(x, wqkv, wo, w1, w2, (u16*)d_out, (unsigned char*)d_ws);
}
```

```cpp
#include <hip/hip_runtime.h>
#include <stdint.h>
#include <utility>

namespace {

typedef unsigned v4u __attribute__((ext_vector_type(4)));
typedef unsigned v2u __attribute__((ext_vector_type(2)));
typedef float f32x4 __attribute__((ext_vector_type(4)));
typedef float f32x2 __attribute__((ext_vector_type(2)));
typedef __bf16 bf2 __attribute__((ext_vector_type(2)));
typedef __bf16 bf8 __attribute__((ext_vector_type(8)));
typedef short s4v __attribute__((ext_vector_type(4)));
typedef unsigned short u16;
#define DI __device__ __forceinline__
#define GAS __attribute__((address_space(1)))
#define LAS __attribute__((address_space(3)))
typedef GAS unsigned gu32;
#define RLX_AGENT __ATOMIC_RELAXED, __HIP_MEMORY_SCOPE_AGENT
typedef __amdgpu_buffer_rsrc_t rsrc_t;

constexpr unsigned OFF_FLAGS = 0;
constexpr unsigned OFF_WQKV = 100352;
constexpr unsigned OFF_WO = OFF_WQKV + 1536u * 1024u;
constexpr unsigned OFF_W1 = OFF_WO + 512u * 1024u;
constexpr unsigned OFF_W2 = OFF_W1 + 1792u * 1024u;
constexpr unsigned OFF_CS1 = OFF_W2 + 1792u * 1024u;
constexpr unsigned OFF_CSQ = OFF_CS1 + 8u * 1792u * 4u;
constexpr unsigned OFF_PQ = OFF_CSQ + 16u * 96u * 4u;
constexpr unsigned KVREC = 2304;
constexpr unsigned OFF_KVP = OFF_PQ + 256u * 8192u;
constexpr unsigned OFF_ST = OFF_KVP + 2u * 256u * KVREC;
constexpr unsigned ST_BYTES = 128u * 1024u;
constexpr unsigned OFF_O = OFF_ST + 3u * ST_BYTES;
constexpr unsigned XF_BYTES = 128u * 16u * 1024u;
constexpr unsigned OFF_X1 = OFF_O + 2u * XF_BYTES;
constexpr unsigned OFF_X2 = OFF_X1 + 2u * XF_BYTES;
constexpr unsigned HF_BYTES = 128u * 56u * 1024u;
constexpr unsigned OFF_H = OFF_X2 + XF_BYTES;
constexpr unsigned WS_BYTES = OFF_H + 2u * HF_BYTES;
static_assert(OFF_O % 256 == 0 && OFF_KVP % 256 == 0 && OFF_PQ % 256 == 0 && OFF_ST % 256 == 0, "align");

constexpr unsigned FO_PRE = 0, FO_EPOCH = 1024, FO_TMO = 1088, FO_CNT = 2048;
constexpr int FC_O = 0;
constexpr int FC_X1 = 128;
constexpr int FC_H = 384;
constexpr int FC_X2 = 512;
constexpr int FC_KV = 640;
constexpr unsigned FLAG_BYTES = 2048 + 256 * 384;
static_assert(FO_CNT + (FC_KV + 64) * 128 <= FLAG_BYTES, "flags");
constexpr unsigned SPIN_CAP = 1u << 17;

constexpr int AUX_SC1 = 16;

constexpr unsigned LU = 16384;
constexpr unsigned L_SCR = 9 * LU;
constexpr unsigned L_CS1 = L_SCR;
constexpr unsigned L_CSQ = L_SCR + 512;
constexpr unsigned L_KVX = L_SCR + 1024;
constexpr unsigned L_CSP = L_SCR + 9728;
constexpr unsigned L_STX = L_SCR + 11264;
constexpr unsigned L_STAMP = L_SCR + 12288;
constexpr unsigned L_XB = 6 * LU;
constexpr int W1U[2][7] = {{4, 5, 6, 7, 8, 0, 1}, {0, 1, 8, 6, 7, 2, 3}};
constexpr int WOU[2] = {0, 2};
constexpr int QU = 2;
constexpr int REDU[2][3] = {{0, 1, 8}, {4, 5, 6}};
constexpr unsigned K2_LDS = 10 * LU;

DI f32x4 mfma(v4u a, v4u b, f32x4 c) {
  return __builtin_amdgcn_mfma_f32_16x16x32_bf16(__builtin_bit_cast(bf8, a), __builtin_bit_cast(bf8, b), c, 0, 0, 0);
}
template <bool FIRST> DI void mfma2(f32x4& c0, f32x4& c1, v4u a0, v4u b0, v4u a1, v4u b1) {
  if constexpr (FIRST)
    asm volatile("v_mfma_f32_16x16x32_bf16 %0, %2, %3, 0\n\tv_mfma_f32_16x16x32_bf16 %1, %4, %5, 0"
                 : "=&v"(c0), "=&v"(c1) : "v"(a0), "v"(b0), "v"(a1), "v"(b1));
  else
    asm volatile("v_mfma_f32_16x16x32_bf16 %0, %2, %3, %0\n\tv_mfma_f32_16x16x32_bf16 %1, %4, %5, %1"
                 : "+v"(c0), "+v"(c1) : "v"(a0), "v"(b0), "v"(a1), "v"(b1));
}
template <bool FIRST> DI void mfma1(f32x4& c0, v4u a0, v4u b0) {
  if constexpr (FIRST) asm volatile("v_mfma_f32_16x16x32_bf16 %0, %1, %2, 0" : "=&v"(c0) : "v"(a0), "v"(b0));
  else asm volatile("v_mfma_f32_16x16x32_bf16 %0, %1, %2, %0" : "+v"(c0) : "v"(a0), "v"(b0));
}
template <bool FIRST> DI void mfma4a(f32x4 (&c)[4], v4u w0, v4u w1, v4u w2, v4u w3, v4u b) {
  if constexpr (FIRST)
    asm volatile("s_nop 1\n\tv_mfma_f32_16x16x32_bf16 %0, %4, %8, 0\n\tv_mfma_f32_16x16x32_bf16 %1, %5, %8, 0\n\t"
                 "v_mfma_f32_16x16x32_bf16 %2, %6, %8, 0\n\tv_mfma_f32_16x16x32_bf16 %3, %7, %8, 0"
                 : "=&v"(c[0]), "=&v"(c[1]), "=&v"(c[2]), "=&v"(c[3]) : "a"(w0), "a"(w1), "a"(w2), "a"(w3), "v"(b));
  else
    asm volatile("s_nop 1\n\tv_mfma_f32_16x16x32_bf16 %0, %4, %8, %0\n\tv_mfma_f32_16x16x32_bf16 %1, %5, %8, %1\n\t"
                 "v_mfma_f32_16x16x32_bf16 %2, %6, %8, %2\n\tv_mfma_f32_16x16x32_bf16 %3, %7, %8, %3"
                 : "+v"(c[0]), "+v"(c[1]), "+v"(c[2]), "+v"(c[3]) : "a"(w0), "a"(w1), "a"(w2), "a"(w3), "v"(b));
}
#define MFMA_FENCE() asm volatile("s_nop 15\n\ts_nop 3" ::: "memory")
DI void pinv(f32x4& x) { asm volatile("" : "+v"(x)); }
#define PINV(x) pinv(x)
DI unsigned pack2(float a, float b) {
  f32x2 v = {a, b};
  return __builtin_bit_cast(unsigned, __builtin_convertvector(v, bf2));
}
DI float bflo(unsigned u) { return __builtin_bit_cast(float, u << 16); }
DI float bfhi(unsigned u) { return __builtin_bit_cast(float, u & 0xffff0000u); }
DI float dot2(unsigned a, unsigned b, float c) {
  return __builtin_amdgcn_fdot2_f32_bf16(__builtin_bit_cast(bf2, a), __builtin_bit_cast(bf2, b), c, false);
}
constexpr float LOG2E = 1.4426950408889634f;
DI float phi(float t) { return __builtin_amdgcn_exp2f(fminf(t, 0.f) * LOG2E) + (fmaxf(t, 0.f) + 1e-6f); }
DI float silu(float t) { return t * __builtin_amdgcn_rcpf(1.0f + __builtin_amdgcn_exp2f(t * -LOG2E)); }
DI v4u ldb(rsrc_t rs, unsigned off) { return __builtin_amdgcn_raw_buffer_load_b128(rs, off, 0, 0); }
DI v4u ldb1(rsrc_t rs, unsigned off) { return __builtin_amdgcn_raw_buffer_load_b128(rs, off, 0, AUX_SC1); }
DI v4u lda128(v4u srd, unsigned off) {
  v4u t;
  asm volatile("buffer_load_dwordx4 %0, %1, %2, 0 offen sc1" : "=a"(t) : "v"(off), "s"(srd) : "memory");
  return t;
}
DI void stb1(rsrc_t rs, unsigned off, v4u v) { __builtin_amdgcn_raw_buffer_store_b128(v, rs, off, 0, AUX_SC1); }
DI void stb1_64(rsrc_t rs, unsigned off, v2u v) { __builtin_amdgcn_raw_buffer_store_b64(v, rs, off, 0, AUX_SC1); }
#define DRAIN() asm volatile("s_waitcnt vmcnt(0)" ::: "memory")
#define LGKM0() asm volatile("s_waitcnt lgkmcnt(0)" ::: "memory")
#define BAR() do { asm volatile("s_waitcnt lgkmcnt(0)" ::: "memory"); __builtin_amdgcn_s_barrier(); asm volatile("" ::: "memory"); } while (0)
#define SCHED() __builtin_amdgcn_sched_barrier(0)
#define PIN(x) asm volatile("" : "+v"(x))
#define PIN4(a, o) asm volatile("" : "+v"(a[(o)]), "+v"(a[(o) + 1]), "+v"(a[(o) + 2]), "+v"(a[(o) + 3]))
#define STAMP(i) do {} while (0)
#define STAMP_INIT() do {} while (0)
#define STAMP_FLUSH(lo, hi) do {} while (0)

template <int... I, typename F> DI void static_for_impl(std::integer_sequence<int, I...>, F&& f) { (f(std::integral_constant<int, I>{}), ...); }
template <int N, typename F> DI void static_for(F&& f) { static_for_impl(std::make_integer_sequence<int, N>{}, f); }

DI void dma16(const void* sbase, unsigned voff, unsigned lds_dst) {
  unsigned keep;
  asm volatile("s_mov_b32 %0, m0\n\ts_mov_b32 m0, %3\n\ts_nop 0\n\tglobal_load_lds_dwordx4 %1, %2\n\ts_mov_b32 m0, %0"
               : "=&s"(keep) : "v"(voff), "s"(sbase), "s"(lds_dst) : "memory");
}
DI v4u tr_frag(LAS unsigned char* p) {
  s4v lo = __builtin_amdgcn_ds_read_tr16_b64_v4i16((LAS s4v*)(p));
  s4v hi = __builtin_amdgcn_ds_read_tr16_b64_v4i16((LAS s4v*)(p + 256));
  v2u l2 = __builtin_bit_cast(v2u, lo), h2 = __builtin_bit_cast(v2u, hi);
  return v4u{l2.x, l2.y, h2.x, h2.y};
}
DI float sum16(float v) {
  const unsigned u = __builtin_bit_cast(unsigned, v);
  unsigned u2 = u; PIN(u2);
  auto r = __builtin_amdgcn_permlane16_swap(u, u2, false, false);
  const unsigned a = r[0], b = r[1];
  return __builtin_bit_cast(float, a) + __builtin_bit_cast(float, b);
}
DI float sum32(float v) {
  const unsigned u = __builtin_bit_cast(unsigned, v);
  unsigned u2 = u; PIN(u2);
  auto r = __builtin_amdgcn_permlane32_swap(u, u2, false, false);
  const unsigned a = r[0], b = r[1];
  return __builtin_bit_cast(float, a) + __builtin_bit_cast(float, b);
}
DI float sum_q(float v) { return sum32(sum16(v)); }
DI v4u pair16(unsigned a0, unsigned a1, unsigned b0, unsigned b1) {
  auto r0 = __builtin_amdgcn_permlane16_swap(a0, b0, false, false);
  auto r1 = __builtin_amdgcn_permlane16_swap(a1, b1, false, false);
  const unsigned x0 = r0[0], x1 = r1[0], x2 = r0[1], x3 = r1[1];
  return v4u{x0, x1, x2, x3};
}

DI bool spin_fail(unsigned& sp, gu32* fb, unsigned epoch) {
  if (++sp > SPIN_CAP) { __hip_atomic_store(fb + FO_TMO / 4, epoch, RLX_AGENT); return true; }
  return (sp & 255u) == 0u && __hip_atomic_load(fb + FO_TMO / 4, RLX_AGENT) == epoch;
}
DI void poll_ge(gu32* fb, int line, unsigned want, unsigned epoch) {
  gu32* wd = fb + (FO_CNT / 4) + line * 32;
  for (unsigned sp = 0;;) {
    if ((unsigned)__builtin_amdgcn_readfirstlane(__hip_atomic_load(wd, RLX_AGENT)) >= want) return;
    __builtin_amdgcn_s_sleep(1);
    if (spin_fail(sp, fb, epoch)) return;
  }
}
DI void poll_ge2(gu32* fb, int line, unsigned want, unsigned epoch, int lane) {
  gu32* wd = fb + (FO_CNT / 4) + line * 32 + (lane & 1);
  for (unsigned sp = 0;;) {
    if (__all(__hip_atomic_load(wd, RLX_AGENT) >= want)) return;
    __builtin_amdgcn_s_sleep(1);
    if (spin_fail(sp, fb, epoch)) return;
  }
}
DI void arrive(gu32* fb, int line, int dw, int lane) { if (lane == 0) __hip_atomic_fetch_add(fb + (FO_CNT / 4) + line * 32 + dw, 1u, RLX_AGENT); }

struct Stats { float a[2], b[2]; };
DI void finish_stats(float s1, float s2, float& a, float& b) {
  const float mean = s1 * (1.0f / 512.0f);
  const float var = fmaxf(s2 * (1.0f / 512.0f) - mean * mean, 0.f);
  a = __builtin_amdgcn_rsqf(var + 1e-5f);
  b = -mean * a;
}

DI void kv_epilogue(const f32x4 (&acc)[6][2], LAS unsigned char* stx, LAS float* csq, int lane, f32x4& kva, f32x4& kvb, f32x4& za, f32x4& zb) {
  const int q = lane >> 4, n = lane & 15;
  f32x4 ab[2][2];
#pragma unroll
  for (int rt = 0; rt < 2; ++rt) { ab[rt][0] = *(LAS f32x4*)(stx + rt * 128 + q * 32); ab[rt][1] = *(LAS f32x4*)(stx + rt * 128 + q * 32 + 16); }
  v4u PK[2], V[2];
#pragma unroll
  for (int h = 0; h < 2; ++h) {
    const float ck = csq[(2 * h) * 16 + n], cv = csq[(2 * h + 1) * 16 + n];
    unsigned pk[4], pv[4];
#pragma unroll
    for (int rt = 0; rt < 2; ++rt) {
      float a[4], b[4];
#pragma unroll
      for (int r = 0; r < 4; ++r) {
        const float sa = ab[rt][r >> 1][(r & 1) * 2], sb = ab[rt][r >> 1][(r & 1) * 2 + 1];
        a[r] = phi(fmaf(acc[2 * h][rt][r], sa, sb * ck));
        b[r] = fmaf(acc[2 * h + 1][rt][r], sa, sb * cv);
      }
      pk[2 * rt] = pack2(a[0], a[1]); pk[2 * rt + 1] = pack2(a[2], a[3]);
      pv[2 * rt] = pack2(b[0], b[1]); pv[2 * rt + 1] = pack2(b[2], b[3]);
    }
    PK[h] = v4u{pk[0], pk[1], pk[2], pk[3]};
    V[h] = v4u{pv[0], pv[1], pv[2], pv[3]};
  }
  const f32x4 z4 = {0, 0, 0, 0};
  const v4u ones = {0x3f803f80u, 0x3f803f80u, 0x3f803f80u, 0x3f803f80u};
  kva = mfma(PK[0], V[0], z4); kvb = mfma(PK[1], V[1], z4);
  za = mfma(PK[0], ones, z4); zb = mfma(PK[1], ones, z4);
}
DI void q_epilogue(const f32x4 (&acc)[6][2], const Stats& st, LAS float* csq, int lane, v4u (&pq)[2]) {
  const int q = lane >> 4;
#pragma unroll
  for (int rt = 0; rt < 2; ++rt) {
    unsigned p[4];
#pragma unroll
    for (int t = 0; t < 2; ++t) {
      const f32x4 c = *(LAS f32x4*)(csq + (4 + t) * 16 + 4 * q);
      float v[4];
#pragma unroll
      for (int r = 0; r < 4; ++r) v[r] = phi(fmaf(acc[4 + t][rt][r], st.a[rt], st.b[rt] * c[r]));
      p[2 * t] = pack2(v[0], v[1]); p[2 * t + 1] = pack2(v[2], v[3]);
    }
    pq[rt] = v4u{p[0], p[1], p[2], p[3]};
  }
}

DI void kv_publish(LAS unsigned char* L, unsigned kvx, rsrc_t rs, unsigned rec_off, int lane, int w, f32x4 kva, f32x4 kvb, f32x4 za, f32x4 zb) {
  LAS unsigned char* me = L + kvx + w * 2176;
  *(LAS f32x4*)(me + lane * 16) = kva;
  *(LAS f32x4*)(me + 1024 + lane * 16) = kvb;
  if ((lane & 15) == 0) {
    *(LAS f32x4*)(me + 2048 + (lane >> 4) * 16) = za;
    *(LAS f32x4*)(me + 2112 + (lane >> 4) * 16) = zb;
  }
  BAR();
  if (w < 2 || (w == 2 && lane < 8)) {
    const unsigned o = w * 1024 + lane * 16;
    f32x4 s = *(LAS f32x4*)(L + kvx + o);
#pragma unroll
    for (int ww = 1; ww < 4; ++ww) s += *(LAS f32x4*)(L + kvx + ww * 2176 + o);
    stb1(rs, rec_off + o, __builtin_bit_cast(v4u, s));
  }
}

struct LBase { LAS unsigned char* b[3]; };
DI v4u ldsf(const LBase& lb, unsigned off) { return *(LAS v4u*)(lb.b[off >> 16] + (off & 0xffffu)); }

DI void publish(gu32* fb, int line, int tid) {
  DRAIN();
  BAR();
  arrive(fb, line, 0, tid);
}
template <int NKB>
DI void dma_block(const unsigned char* ws, unsigned src, unsigned ldst, int w, int lane) {
  static_assert(NKB % 16 == 0, "");
  const unsigned char* p = ws + src + (unsigned)w * (NKB / 4) * 1024u;
  unsigned d = ldst + (unsigned)w * (NKB / 4) * 1024u;
#pragma unroll 1
  for (int i = 0; i < NKB / 16; ++i) {
    dma16(p, (unsigned)lane * 16u, d);
    dma16(p + 1024, (unsigned)lane * 16u, d + 1024);
    dma16(p + 2048, (unsigned)lane * 16u, d + 2048);
    dma16(p + 3072, (unsigned)lane * 16u, d + 3072);
    p += 4096; d += 4096;
  }
}
template <int NKB>
DI void dma_w123(const unsigned char* ws, unsigned src, unsigned ldst, int w, int lane) {
  if (w == 0) return;
  const unsigned char* p = ws + src + (unsigned)(w - 1) * 1024u;
  unsigned d = ldst + (unsigned)(w - 1) * 1024u;
#pragma unroll 1
  for (int i = w - 1; i < NKB; i += 3) {
    dma16(p, (unsigned)lane * 16u, d);
    p += 3072; d += 3072;
  }
}
template <int NT, int NTN, int AD, int FD, typename FA, typename HOOK>
DI void gemm_k(rsrc_t rs, unsigned ab, const LBase& lb, f32x4 (&acc)[NT][2], FA faddr, HOOK hook) {
  constexpr int R = AD + 1, NF = 16 * NT;
  v4u xa[R], xb[R];
#pragma unroll
  for (int ks = 0; ks < AD; ++ks) { xa[ks] = ldb1(rs, ab + ks * 1024); xb[ks] = ldb1(rs, ab + (16 + ks) * 1024); }
  v4u wf[FD];
#pragma unroll
  for (int i = 0; i < FD; ++i) wf[i] = ldsf(lb, faddr(i % NT, i / NT));
  SCHED();
  static_for<NF>([&](auto I) {
    constexpr int i = I, t = i % NT, ks = i / NT;
    const v4u f = wf[i % FD];
    if constexpr (i + FD < NF) wf[i % FD] = ldsf(lb, faddr((i + FD) % NT, (i + FD) / NT));
    if constexpr (t < NTN) mfma2<ks == 0>(acc[t][0], acc[t][1], xa[ks % R], f, xb[ks % R], f);
    else mfma2<ks == 0>(acc[t][0], acc[t][1], f, xa[ks % R], f, xb[ks % R]);
    if constexpr (t == 1 && ks + AD < 16) { xa[(ks + AD) % R] = ldb1(rs, ab + (ks + AD) * 1024); xb[(ks + AD) % R] = ldb1(rs, ab + (16 + ks + AD) * 1024); }
    hook(I);
    SCHED();
  });
  MFMA_FENCE();
#pragma unroll
  for (int t = 0; t < NT; ++t) { PINV(acc[t][0]); PINV(acc[t][1]); }
}
DI void res_update(v2u (&res)[4], const f32x4 (&acc)[4]) {
#pragma unroll
  for (int c = 0; c < 4; ++c) {
    const unsigned t0 = pack2(acc[c][0], acc[c][1]), t1 = pack2(acc[c][2], acc[c][3]);
    res[c].x = pack2(bflo(res[c].x) + bflo(t0), bfhi(res[c].x) + bfhi(t0));
    res[c].y = pack2(bflo(res[c].y) + bflo(t1), bfhi(res[c].y) + bfhi(t1));
  }
}
DI void store_x_tile(rsrc_t rs, unsigned xoff, unsigned stoff, const v2u (&res)[4], int myrt, int cs, int lane) {
  const int q = lane >> 4, n = lane & 15;
  const unsigned kg = (q & 1) * 2 + (q >> 1);
#pragma unroll
  for (int c2 = 0; c2 < 2; ++c2) {
    const v4u o = pair16(res[2 * c2].x, res[2 * c2].y, res[2 * c2 + 1].x, res[2 * c2 + 1].y);
    stb1(rs, xoff + (unsigned)(myrt * 16 + 2 * cs + c2) * 1024u + (kg * 16 + n) * 16, o);
  }
  float s1 = 0, s2 = 0;
#pragma unroll
  for (int c = 0; c < 4; ++c) {
    s1 = dot2(res[c].x, 0x3f803f80u, s1); s2 = dot2(res[c].x, res[c].x, s2);
    s1 = dot2(res[c].y, 0x3f803f80u, s1); s2 = dot2(res[c].y, res[c].y, s2);
  }
  s1 = sum_q(s1); s2 = sum_q(s2);
  if (q == 0) stb1_64(rs, stoff + (unsigned)myrt * 1024u + n * 64 + cs * 8, __builtin_bit_cast(v2u, f32x2{s1, s2}));
}
DI void load_stats(rsrc_t rs, unsigned stoff, int rt0, int lane, v4u (&sp)[2]) {
  const int q = lane >> 4, n = lane & 15;
#pragma unroll
  for (int rt = 0; rt < 2; ++rt) sp[rt] = ldb1(rs, stoff + (unsigned)(rt0 + rt) * 1024u + n * 64 + q * 16);
}
DI void make_stats(const v4u (&sp)[2], Stats& st) {
#pragma unroll
  for (int rt = 0; rt < 2; ++rt) {
    const f32x4 p = __builtin_bit_cast(f32x4, sp[rt]);
    finish_stats(sum_q(p[0] + p[2]), sum_q(p[1] + p[3]), st.a[rt], st.b[rt]);
  }
}

__global__ void __launch_bounds__(256, 1) kmain(const u16* __restrict__ x, const u16* __restrict__ wqkv, const u16* __restrict__ wo,
                                                const u16* __restrict__ w1, const u16* __restrict__ w2g, u16* __restrict__ out,
                                                unsigned char* __restrict__ ws) {
  extern __shared__ __attribute__((aligned(16))) unsigned char smem[];
  LAS unsigned char* L = (LAS unsigned char*)smem;
  constexpr unsigned LSTAMP = L_STAMP;
  const unsigned lbase = (unsigned)(size_t)L;
  const int tid = threadIdx.x, lane0 = tid & 63, w = __builtin_amdgcn_readfirstlane(tid >> 6);
  const int b = blockIdx.x;
  const rsrc_t rs = __builtin_amdgcn_make_buffer_rsrc((void*)ws, 0, WS_BYTES, 0x00020000);
  const v4u srd = {(unsigned)(size_t)ws, (unsigned)((size_t)ws >> 32) & 0xffffu, WS_BYTES, 0x00020000u};
  gu32* fb = (gu32*)(ws + OFF_FLAGS);
  const f32x4 z4 = {0, 0, 0, 0};
  const int gm = 4 * ((b & 7) >> 1) + (b >> 6), jm = 8 * (b & 1) + ((b >> 3) & 7);
  const int hh = (jm >> 2) & 1, cs = (jm & 3) | ((jm >> 3) << 2);
#define FRESH() int lane = lane0; PIN(lane); const int q = lane >> 4, n = lane & 15; const unsigned l16 = (unsigned)lane * 16u; \
  LBase lb; { unsigned o1 = l16 + 65536u, o2 = l16 + 131072u; PIN(o1); PIN(o2); lb.b[0] = L + l16; lb.b[1] = L + o1; lb.b[2] = L + o2; } (void)q; (void)n; (void)lb; \
  const unsigned char* wsp = ws; asm volatile("" : "+s"(wsp)); int jo = jm; asm volatile("" : "+s"(jo)); (void)wsp; (void)jo;
  STAMP_INIT();
  STAMP(0);
  const unsigned epoch = __hip_atomic_load(fb + FO_EPOCH / 4, RLX_AGENT) + 1u;

  v4u w2[14][4];
  auto ldw2 = [&](auto I, unsigned l16) {
    constexpr int i = I;
    w2[i >> 2][i & 3] = ldb1(rs, OFF_W2 + (unsigned)((cs * 4 + w) * 56 + i) * 1024u + l16);
  };
  {
    FRESH();
    const int g = 4 * ((b & 7) >> 1) + (b >> 6), j = 8 * (b & 1) + ((b >> 3) & 7);
    const int drow = lane >> 2, dch = (lane & 3) ^ (((drow >> 3) & 1) << 1);
    {
      const unsigned voff = (unsigned)(drow * 1536 + 32 * j + dch * 8) * 2u;
#pragma unroll
      for (int i = 0; i < 24; ++i) {
        const int it = 4 * i + w, rb = it / 3, seg = it % 3;
        dma16((const unsigned char*)wqkv + (size_t)(rb * 16 * 1536 + seg * 512) * 2, voff, lbase + (seg * 32 + rb) * 1024);
      }
    }
    v4u xr[4][8];
    {
      const GAS v4u* xp = (const GAS v4u*)(x + (size_t)(128 * g + 32 * w + (lane >> 5)) * 512 + (lane & 31) * 8);
#pragma unroll
      for (int blk = 0; blk < 4; ++blk)
#pragma unroll
        for (int i = 0; i < 8; ++i) xr[blk][i] = xp[((blk >> 1) * 16 + 2 * i) * 64 + (blk & 1) * 32];
    }
    const u16* psrc; int pld, pp, pks, pkind, prow[4];
    if (b < 112) { psrc = w1; pld = 1792; pp = b >> 1; pks = 8 * (b & 1) + 2 * w; pkind = 0; }
    else {
      const int u = (b - 112) * 8 + 2 * w;
      if (u < 896) { psrc = w2g; pld = 512; pp = u / 56; pks = u % 56; pkind = 1; }
      else { psrc = wo; pld = 512; pp = (u - 896) >> 4; pks = (u - 896) & 15; pkind = 2; }
    }
#pragma unroll
    for (int i = 0; i < 4; ++i) {
      const int ks = pks + (i >> 1);
      prow[i] = pkind == 1 ? 112 * (2 * (ks / 7) + (i & 1)) + 16 * (ks % 7) : 32 * ks + 16 * (i & 1);
    }
    v4u pt[4];
#pragma unroll
    for (int i = 0; i < 4; ++i) pt[i] = *(const GAS v4u*)(psrc + (size_t)(prow[i] + drow) * pld + 32 * pp + dch * 8);
    SCHED();
    if (tid < 24) stb1(rs, OFF_FLAGS + FO_CNT + b * 384 + tid * 16, v4u{0u, 0u, 0u, 0u});
    STAMP(1);

    v4u xa[16], xb[16];
    float s1a = 0, s2a = 0, s1b = 0, s2b = 0;
    LAS unsigned char* B = L + L_XB + w * 8192;
    {
      const int c = lane & 31, ksl = c >> 2, kgw = c & 3, fw = (ksl & 1) * 4 + kgw;
      LAS unsigned char* wp = B + ksl * 1024 + kgw * 256;
      LAS unsigned char* rp0 = B + q * 256 + ((n ^ q) * 16);
      LAS unsigned char* rp1 = B + q * 256 + ((n ^ (4 + q)) * 16);
#pragma unroll
      for (int blk = 0; blk < 4; ++blk) {
#pragma unroll
        for (int i = 0; i < 8; ++i) *(LAS v4u*)(wp + (((2 * i + (lane >> 5)) ^ fw) * 16)) = xr[blk][i];
        LGKM0();
#pragma unroll
        for (int k8 = 0; k8 < 8; ++k8) {
          const v4u f = *(LAS v4u*)(((k8 & 1) ? rp1 : rp0) + k8 * 1024);
          if (blk < 2) xa[(blk & 1) * 8 + k8] = f; else xb[(blk & 1) * 8 + k8] = f;
#pragma unroll
          for (int e = 0; e < 4; ++e) {
            if (blk < 2) { s1a = dot2(f[e], 0x3f803f80u, s1a); s2a = dot2(f[e], f[e], s2a); }
            else { s1b = dot2(f[e], 0x3f803f80u, s1b); s2b = dot2(f[e], f[e], s2b); }
          }
        }
        PIN(s1a); PIN(s2a); PIN(s1b); PIN(s2b);
        LGKM0();
      }
    }
    STAMP(2);
    const int r4 = n >> 2, c4 = n & 3;
    unsigned trb[2];
#pragma unroll
    for (int ct = 0; ct < 2; ++ct)
      trb[ct] = (unsigned)((q >> 1) * 1024 + ((q & 1) * 8 + r4) * 64 + (2 * (ct ^ (q & 1)) + (c4 >> 1)) * 16 + (c4 & 1) * 8);
    const v4u ones = {0x3f803f80u, 0x3f803f80u, 0x3f803f80u, 0x3f803f80u};
    {
#pragma unroll
      for (int i = 0; i < 4; ++i) *(LAS v4u*)(B + i * 1024 + l16) = pt[i];
      LGKM0();
      f32x4 c1[2] = {z4, z4};
#pragma unroll
      for (int u = 0; u < 2; ++u)
#pragma unroll
        for (int ct = 0; ct < 2; ++ct) {
          const v4u f = tr_frag(B + u * 2048 + trb[ct]);
          const int gct = 2 * pp + ct, ks = pks + u;
          unsigned dst;
          if (pkind == 0) dst = OFF_W1 + (unsigned)((gct / 7) * 112 + (gct % 7) * 16 + ks) * 1024u;
          else if (pkind == 1) { const int p = ks / 7, t = ks % 7; dst = OFF_W2 + (unsigned)(((gct >> 2) * 4 + (p >> 1)) * 56 + (2 * t + (p & 1)) * 4 + (gct & 3)) * 1024u; }
          else dst = OFF_WO + (unsigned)((gct >> 2) * 64 + (gct & 3) * 16 + ks) * 1024u;
          stb1(rs, dst + l16, f);
          c1[ct] = mfma(f, ones, c1[ct]);
        }
      if (pkind == 0 && n == 0) {
        const int part = (b & 1) * 4 + w;
#pragma unroll
        for (int ct = 0; ct < 2; ++ct)
          stb1(rs, OFF_CS1 + (unsigned)(part * 1792 + 32 * pp + 16 * ct + 4 * q) * 4u, __builtin_bit_cast(v4u, c1[ct]));
      }
    }
    Stats st;
    finish_stats(sum_q(s1a), sum_q(s2a), st.a[0], st.b[0]);
    finish_stats(sum_q(s1b), sum_q(s2b), st.a[1], st.b[1]);
    if (q == 0) {
      *(LAS f32x2*)(L + L_STX + w * 256 + n * 8) = f32x2{st.a[0], st.b[0]};
      *(LAS f32x2*)(L + L_STX + w * 256 + 128 + n * 8) = f32x2{st.a[1], st.b[1]};
    }
    STAMP(3);
    DRAIN();
    BAR();
    STAMP(4);
    {
      f32x4 cs[6] = {z4, z4, z4, z4, z4, z4};
#pragma unroll
      for (int i = 0; i < 4; ++i)
#pragma unroll
        for (int seg = 0; seg < 3; ++seg) {
          LAS unsigned char* blk = L + seg * 32768 + (4 * w + i) * 2048;
          const v4u f0 = tr_frag(blk + trb[0]), f1 = tr_frag(blk + trb[1]);
          const int t0 = seg == 0 ? 4 : seg - 1, t1 = seg == 0 ? 5 : seg + 1;
          if (seg == 0) { cs[4] = mfma(f0, ones, cs[4]); cs[5] = mfma(f1, ones, cs[5]); }
          else { cs[seg - 1] = mfma(ones, f0, cs[seg - 1]); cs[seg + 1] = mfma(ones, f1, cs[seg + 1]); }
          *(LAS v4u*)(blk + l16) = f0;
          *(LAS v4u*)(blk + 1024 + l16) = f1;
          if (4 * w + i == g) {
            stb1(rs, OFF_WQKV + (unsigned)(j * 96 + t0 * 16 + g) * 1024u + l16, f0);
            stb1(rs, OFF_WQKV + (unsigned)(j * 96 + t1 * 16 + g) * 1024u + l16, f1);
          }
        }
      LAS float* csw = (LAS float*)(L + L_CSP) + w * 96;
#pragma unroll
      for (int t = 4; t < 6; ++t) if (n == 0) *(LAS f32x4*)(csw + t * 16 + 4 * q) = cs[t];
#pragma unroll
      for (int t = 0; t < 4; ++t) if (q == 0) csw[t * 16 + n] = cs[t][0];
    }
    BAR();
    if (tid < 96) {
      LAS float* c = (LAS float*)(L + L_CSP);
      const float s = (c[tid] + c[96 + tid]) + (c[192 + tid] + c[288 + tid]);
      ((LAS float*)(L + L_CSQ))[tid] = s;
      if (g == 0) __hip_atomic_store((GAS float*)(ws + OFF_CSQ) + j * 96 + tid, s, RLX_AGENT);
    }
    DRAIN();
    BAR();
    if (tid == 0) __hip_atomic_store(fb + FO_PRE / 4 + b, epoch, RLX_AGENT);
    f32x4 acc[6][2];
    {
      constexpr int FD = 6, NF = 96;
      auto addr = [&](int i) -> unsigned {
        const int t = i % 6, ks = i / 6;
        const int seg = t < 4 ? 1 + (t & 1) : 0, ct = t < 4 ? (t >> 1) : (t - 4);
        return (unsigned)(seg * 32768 + ks * 2048 + ct * 1024);
      };
      v4u wf[FD];
#pragma unroll
      for (int i = 0; i < FD; ++i) wf[i] = ldsf(lb, addr(i));
      SCHED();
      static_for<NF>([&](auto I) {
        constexpr int i = I, t = i % 6, ks = i / 6;
        const v4u f = wf[i % FD];
        if constexpr (i + FD < NF) wf[i % FD] = ldsf(lb, addr(i + FD));
        if constexpr (t < 4) mfma2<ks == 0>(acc[t][0], acc[t][1], xa[ks], f, xb[ks], f);
        else mfma2<ks == 0>(acc[t][0], acc[t][1], f, xa[ks], f, xb[ks]);
        SCHED();
      });
    }
    MFMA_FENCE();
#pragma unroll
    for (int t = 0; t < 6; ++t) { PINV(acc[t][0]); PINV(acc[t][1]); }
    STAMP(5);
    f32x4 kva, kvb, za, zb;
    kv_epilogue(acc, L + L_STX + w * 256, (LAS float*)(L + L_CSQ), lane, kva, kvb, za, zb);
    v4u pq0[2];
    q_epilogue(acc, st, (LAS float*)(L + L_CSQ), lane, pq0);
    const int cu = g * 16 + j;
#pragma unroll
    for (int rt = 0; rt < 2; ++rt) stb1(rs, OFF_PQ + (unsigned)((cu * 4 + w) * 2 + rt) * 1024u + l16, pq0[rt]);
    if (w == 0) {
      for (unsigned sp = 0;;) {
        const v4u v = ldb1(rs, OFF_FLAGS + FO_PRE + l16);
        if (__all(v.x == epoch && v.y == epoch && v.z == epoch && v.w == epoch)) break;
        __builtin_amdgcn_s_sleep(4);
        if (spin_fail(sp, fb, epoch)) break;
      }
    }
    kv_publish(L, L_KVX, rs, OFF_KVP + (unsigned)(j * 16 + g) * KVREC, lane, w, kva, kvb, za, zb);
    STAMP(6);
    publish(fb, FC_KV + 2 * j, tid);
    STAMP(7);
  }

  const int g = gm, j = jm;
  const int cu = g * 16 + j;
  const int myrt = 8 * g + 4 * hh + w;
  const int rt0 = 8 * g + 2 * w;
  v4u pq[2];
  v2u res[4];
  dma_w123<64>(ws, OFF_WO + cs * 65536u, lbase + WOU[0] * LU, w, lane0);
  dma_w123<32>(ws, OFF_W1 + j * 114688u, lbase + W1U[0][0] * LU, w, lane0);
  static_for<14>([&](auto I) { ldw2(I, (unsigned)lane0 * 16u); });
  {
    FRESH();
    const GAS v2u* xp = (const GAS v2u*)(x + (size_t)(16 * myrt + n) * 512 + 64 * cs + 4 * q);
#pragma unroll
    for (int c = 0; c < 4; ++c) res[c] = xp[c * 4];
    if (tid < 112) {
      float csv = 0.f;
#pragma unroll
      for (int i = 0; i < 8; ++i) csv += __hip_atomic_load((GAS float*)(ws + OFF_CS1) + i * 1792 + 112 * j + tid, RLX_AGENT);
      ((LAS float*)(L + L_CS1))[tid] = csv;
    } else if (tid >= 128 && tid < 224) {
      ((LAS float*)(L + L_CSQ))[tid - 128] = __hip_atomic_load((GAS float*)(ws + OFF_CSQ) + j * 96 + tid - 128, RLX_AGENT);
    }
  }
  using std::integral_constant;
#define IC(x) integral_constant<int, (x)>{}
#define VMW(n) asm volatile("s_waitcnt vmcnt(%0)" :: "i"(n) : "memory")

  static_for<2>([&](auto LL) {
    constexpr int l = LL;
    constexpr int SB = 10 + l * 24;
    if (w == 0) poll_ge(fb, FC_KV + (l * 16 + j) * 2, 16u, epoch);
    BAR();
    STAMP(SB + 0);
    {
      FRESH();
      if constexpr (l == 0) {
#pragma unroll
        for (int rt = 0; rt < 2; ++rt) pq[rt] = ldb1(rs, OFF_PQ + (unsigned)((cu * 4 + w) * 2 + rt) * 1024u + l16);
      }
      const unsigned rb = OFF_KVP + (unsigned)((l * 16 + j) * 16) * KVREC;
      if (w < 3) {
        const unsigned o = rb + (w < 2 ? (unsigned)w * 1024u + l16 : 2048u + (unsigned)(lane & 7) * 16u);
        v4u a[16];
#pragma unroll
        for (int i = 0; i < 16; ++i) a[i] = ldb1(rs, o + i * KVREC);
        SCHED();
        f32x4 s[4];
#pragma unroll
        for (int i = 0; i < 4; ++i)
          s[i] = (__builtin_bit_cast(f32x4, a[4 * i]) + __builtin_bit_cast(f32x4, a[4 * i + 1])) + (__builtin_bit_cast(f32x4, a[4 * i + 2]) + __builtin_bit_cast(f32x4, a[4 * i + 3]));
        const f32x4 t = (s[0] + s[1]) + (s[2] + s[3]);
        const v2u pk = v2u{pack2(t[0], t[1]), pack2(t[2], t[3])};
        if (w < 2) *(LAS v2u*)(L + L_KVX + w * 512 + lane * 8) = pk;
        else if (lane < 8) *(LAS v2u*)(L + L_KVX + 1024 + lane * 8) = pk;
      }
      BAR();
      const v2u ka = *(LAS v2u*)(L + L_KVX + lane * 8), kb = *(LAS v2u*)(L + L_KVX + 512 + lane * 8);
      const v2u zz = *(LAS v2u*)(L + L_KVX + 1024 + ((n & 1) * 4 + q) * 8);
      const v4u Aa = {ka.x, ka.y, 0u, 0u};
      const v4u Ab = {0u, 0u, kb.x, kb.y};
      const bool za_ = (n & 3) == 0, zb_ = (n & 3) == 1;
      const v4u Az = {za_ ? zz.x : 0u, za_ ? zz.y : 0u, zb_ ? zz.x : 0u, zb_ ? zz.y : 0u};
#pragma unroll
      for (int rt = 0; rt < 2; ++rt) {
        const f32x4 na = mfma(Aa, pq[rt], z4), nb = mfma(Ab, pq[rt], z4), dn = mfma(Az, pq[rt], z4);
        const float ra = __builtin_amdgcn_rcpf(dn[0] + 1e-6f), rb2 = __builtin_amdgcn_rcpf(dn[1] + 1e-6f);
        const v4u o = pair16(pack2(na[0] * ra, na[1] * ra), pack2(na[2] * ra, na[3] * ra), pack2(nb[0] * rb2, nb[1] * rb2), pack2(nb[2] * rb2, nb[3] * rb2));
        const unsigned kg = (q & 1) * 2 + (q >> 1);
        stb1(rs, OFF_O + l * XF_BYTES + (unsigned)((rt0 + rt) * 16 + j) * 1024u + (kg * 16 + n) * 16, o);
      }
      STAMP(SB + 1);
      DRAIN();
      arrive(fb, FC_O + (l * 16 + g) * 4 + w, 0, lane);
      BAR();
      STAMP(SB + 2);
      if constexpr (l == 0) {
        static_for<12>([&](auto K) {
          const unsigned pc = (unsigned)((K & 3) * 4 + w) * 1024u;
          dma16(wsp + OFF_W1 + jo * 114688u + (2 + (K >> 2)) * LU + pc, l16, lbase + W1U[0][2 + (K >> 2)] * LU + pc);
        });
      } else {
        static_for<8>([&](auto K) {
          const unsigned pc = (unsigned)((K & 3) * 4 + w) * 1024u;
          dma16(wsp + OFF_W1 + jo * 114688u + (3 + (K >> 2)) * LU + pc, l16, lbase + W1U[1][3 + (K >> 2)] * LU + pc);
        });
      }
    }
    poll_ge(fb, FC_O + (l * 16 + g) * 4 + ((4 * hh + w) >> 1), 16u, epoch);
    STAMP(SB + 3);
    {
      FRESH();
      v4u a[16];
      const unsigned ab = OFF_O + l * XF_BYTES + (unsigned)(myrt * 16) * 1024u + l16;
      constexpr int AD = 8;
#pragma unroll
      for (int ks = 0; ks < AD; ++ks) a[ks] = ldb1(rs, ab + ks * 1024);
      f32x4 acc[4];
      constexpr int FD = 8;
      v4u wf[FD];
#pragma unroll
      for (int i = 0; i < FD; ++i) wf[i] = ldsf(lb, (WOU[l] + (i & 3)) * LU + (i >> 2) * 1024);
      SCHED();
      static_for<64>([&](auto I) {
        constexpr int i = I;
        const v4u f = wf[i % FD];
        if constexpr (i + FD < 64) wf[i % FD] = ldsf(lb, (WOU[l] + ((i + FD) & 3)) * LU + ((i + FD) >> 2) * 1024);
        mfma1<(i < 4)>(acc[i & 3], f, a[i >> 2]);
        if constexpr ((i & 3) == 1 && (i >> 2) + AD < 16) a[(i >> 2) + AD] = ldb1(rs, ab + ((i >> 2) + AD) * 1024);
        SCHED();
      });
      MFMA_FENCE();
      PINV(acc[0]); PINV(acc[1]); PINV(acc[2]); PINV(acc[3]);
      res_update(res, acc);
      store_x_tile(rs, OFF_X1 + l * XF_BYTES, OFF_ST + l * ST_BYTES, res, myrt, cs, lane);
      STAMP(SB + 4);
      DRAIN();
      arrive(fb, FC_X1 + (l * 16 + g) * 8 + 4 * hh + w, 0, lane);
      BAR();
      STAMP(SB + 5);
      static_for<8>([&](auto K) {
        const unsigned pc = (unsigned)((K & 3) * 4 + w) * 1024u;
        dma16(wsp + OFF_W1 + jo * 114688u + (5 + (K >> 2)) * LU + pc, l16, lbase + W1U[l][5 + (K >> 2)] * LU + pc);
      });
      if constexpr (l == 0) static_for<14>([&](auto I) { ldw2(IC(14 + I), l16); });
    }

    poll_ge(fb, FC_X1 + (l * 16 + g) * 8 + 2 * w, 8u, epoch);
    poll_ge(fb, FC_X1 + (l * 16 + g) * 8 + 2 * w + 1, 8u, epoch);
    STAMP(SB + 6);
    {
      FRESH();
      const int hline_out = FC_H + ((l * 16 + g) * 2 + (w >> 1)) * 2, hline_in = FC_H + ((l * 16 + g) * 2 + hh) * 2;
      const unsigned ab = OFF_X1 + l * XF_BYTES + (unsigned)(rt0 * 16) * 1024u + l16;
      v4u xf[32], sp[2];
      constexpr int AD = 12, FD = 7, NF = 112;
      load_stats(rs, OFF_ST + l * ST_BYTES, rt0, lane, sp);
#pragma unroll
      for (int f = 0; f < AD; ++f) xf[f] = ldb1(rs, ab + f * 1024);
      VMW(AD + 2);
      BAR();
      auto faddr = [&](int i) -> unsigned { return (unsigned)(W1U[l][i % 7] * LU + (i / 7) * 1024); };
      float sa[2], sb[2];
      const unsigned hst = OFF_H + l * HF_BYTES + ((unsigned)rt0 * 56u + (unsigned)(j >> 1) * 7u) * 1024u + ((2 * (j & 1) + (q >> 1)) * 16 + n) * 16;
      v4u ho[2][4];
      auto half1 = [&](auto HH, auto hook) {
        constexpr int H = HH;
        f32x4 acc[7];
        v4u wf[FD];
#pragma unroll
        for (int i = 0; i < FD; ++i) wf[i] = ldsf(lb, faddr(i));
        SCHED();
        static_for<NF>([&](auto I) {
          constexpr int i = I, t = i % 7, ks = i / 7;
          const v4u f = wf[i % FD];
          if constexpr (i + FD < NF) wf[i % FD] = ldsf(lb, faddr(i + FD));
          mfma1<ks == 0>(acc[t], f, xf[H * 16 + ks]);
          hook(I);
          SCHED();
        });
        MFMA_FENCE();
#pragma unroll
        for (int t = 0; t < 7; ++t) PINV(acc[t]);
        if constexpr (H == 0) {
          Stats st;
          make_stats(sp, st);
          sa[0] = st.a[0] * LOG2E; sa[1] = st.a[1] * LOG2E; sb[0] = st.b[0] * LOG2E; sb[1] = st.b[1] * LOG2E;
        }
        unsigned y2[7][2];
#pragma unroll
        for (int t = 0; t < 7; ++t) {
          const f32x4 c = *(LAS f32x4*)(L + L_CS1 + (t * 16 + 4 * q) * 4);
          float y[4];
#pragma unroll
          for (int r = 0; r < 4; ++r) {
            const float v = fmaf(acc[t][r], sa[H], sb[H] * c[r]);
            y[r] = v * __builtin_amdgcn_rcpf(fmaf(__builtin_amdgcn_exp2f(-v), LOG2E, LOG2E));
          }
          y2[t][0] = pack2(y[0], y[1]); y2[t][1] = pack2(y[2], y[3]);
        }
#pragma unroll
        for (int p = 0; p < 3; ++p) ho[H][p] = pair16(y2[2 * p][0], y2[2 * p][1], y2[2 * p + 1][0], y2[2 * p + 1][1]);
        ho[H][3] = v4u{y2[6][0], y2[6][1], 0u, 0u};
      };
      auto hstore = [&](auto HH) {
        constexpr int H = HH;
#pragma unroll
        for (int p = 0; p < 3; ++p) stb1(rs, hst + H * 56 * 1024 + (2 * p + (q & 1)) * 1024, ho[H][p]);
        stb1_64(rs, hst + H * 56 * 1024 + 6 * 1024 + (q & 1) * 8, v2u{ho[H][3].x, ho[H][3].y});
      };
      half1(IC(0), [&](auto I) {
        constexpr int i = I;
        if constexpr (i % 4 == 2 && AD + i / 4 < 32) xf[AD + i / 4] = ldb1(rs, ab + (AD + i / 4) * 1024);
        if constexpr (i == 0) STAMP(SB + 16);
      });
      STAMP(SB + 17);
      hstore(IC(0));
      SCHED();
      half1(IC(1), [&](auto I) {
        constexpr int i = I;
        if constexpr (i == 72) { VMW(0); arrive(fb, hline_out + 0, w & 1, lane); }
        if constexpr (l == 0 && i >= 76 && i < 104) ldw2(IC(28 + i - 76), l16);
      });
      STAMP(SB + 18);
      constexpr int WIN = 16;
      v4u a[WIN];
      f32x4 acc2[2][4];
      auto hoff = [](int f) -> unsigned { return (unsigned)(((f >> 1) & 1) * 7 + (f >> 2)) * 1024u + (unsigned)(f & 1) * (2 * 56 * 1024); };
      auto hbase = [&](int H) -> unsigned { return OFF_H + l * HF_BYTES + (unsigned)((8 * g + 4 * hh + H) * 56 + 14 * w) * 1024u + l16; };
      auto gemm2_start = [&](auto HH) {
        poll_ge2(fb, hline_in + HH, 16u, epoch, lane);
        const unsigned hb = hbase(HH);
#pragma unroll
        for (int f = 0; f < WIN; ++f) a[f] = ldb1(rs, hb + hoff(f));
      };
      auto gemm2 = [&](auto HH, auto hook) {
        const unsigned hb = hbase(HH);
        static_for<28>([&](auto F) {
          constexpr int f = F, wi = 2 * (f >> 2) + ((f >> 1) & 1);
          const v4u cur = a[f % WIN];
          if constexpr (f + WIN < 28) a[f % WIN] = ldb1(rs, hb + hoff(f + WIN));
          mfma4a<(f < 2)>(acc2[f & 1], w2[wi][0], w2[wi][1], w2[wi][2], w2[wi][3], cur);
          hook(F);
          SCHED();
        });
        MFMA_FENCE();
#pragma unroll
        for (int i2 = 0; i2 < 2; ++i2)
#pragma unroll
          for (int c = 0; c < 4; ++c) PINV(acc2[i2][c]);
      };
      auto reduce2 = [&](auto HH) {
        constexpr int H = HH;
#pragma unroll
        for (int i2 = 0; i2 < 2; ++i2) {
          const int own = H + 2 * i2;
          if (w != own) {
            const int slot = w - (w > own ? 1 : 0);
#pragma unroll
            for (int c = 0; c < 4; ++c) *(LAS f32x4*)(L + REDU[l][i2] * LU + (slot * 4 + c) * 1024 + l16) = acc2[i2][c];
          }
        }
        BAR();
        if ((w & 1) == H) {
          f32x4 s[4];
          const unsigned ru = (w >> 1) ? (unsigned)REDU[l][1] * LU : (unsigned)REDU[l][0] * LU;
#pragma unroll
          for (int c = 0; c < 4; ++c) s[c] = (w >> 1) ? acc2[1][c] : acc2[0][c];
#pragma unroll
          for (int sl = 0; sl < 3; ++sl)
#pragma unroll
            for (int c = 0; c < 4; ++c) s[c] += *(LAS f32x4*)(L + ru + (sl * 4 + c) * 1024 + l16);
          res_update(res, s);
          if constexpr (l == 0) {
            store_x_tile(rs, OFF_X2, OFF_ST + 2 * ST_BYTES, res, myrt, cs, lane);
          } else {
#pragma unroll
            for (int c2 = 0; c2 < 2; ++c2) {
              const v4u o = pair16(res[2 * c2].x, res[2 * c2].y, res[2 * c2 + 1].x, res[2 * c2 + 1].y);
              *(GAS v4u*)(out + (size_t)(16 * myrt + n) * 512 + 64 * cs + 16 * (2 * c2 + (q & 1)) + 8 * (q >> 1)) = o;
            }
          }
        }
      };
      gemm2_start(IC(0));
      hstore(IC(1));
      STAMP(SB + 7);
      BAR();
      gemm2(IC(0), [&](auto F) {
        constexpr int f = F;
        if constexpr (f == 14) { VMW(12 + (l == 0 ? 5 : 0)); arrive(fb, hline_out + 1, w & 1, lane); }
        if constexpr (l == 0 && f >= 4 && (f & 1) == 0) {
          const unsigned o = (unsigned)(((f - 4) >> 1) * 4 + w) * 1024u;
          dma16(wsp + OFF_WQKV + jo * 98304u + o, l16, lbase + QU * LU + o);
        }
      });
      STAMP(SB + 9);
      gemm2_start(IC(1));
      reduce2(IC(0));
      STAMP(SB + 10);
      gemm2(IC(1), [&](auto F) {
        constexpr int f = F;
        if constexpr (l == 0 && f == 14) { VMW(17); if ((w & 1) == 0) arrive(fb, FC_X2 + g * 8 + 4 * hh + w, 0, lane); }
        if constexpr (l == 0 && f >= 4 && (f & 1) == 0) {
          const unsigned o = (unsigned)(48 + ((f - 4) >> 1) * 4 + w) * 1024u;
          dma16(wsp + OFF_WQKV + jo * 98304u + o, l16, lbase + QU * LU + o);
        }
      });
      BAR();
      reduce2(IC(1));
      STAMP(SB + 11);
      if constexpr (l == 0) {
        DRAIN();
        if ((w & 1) == 1) arrive(fb, FC_X2 + g * 8 + 4 * hh + w, 0, lane);
      }
    }
    if constexpr (l == 0) {
      poll_ge(fb, FC_X2 + g * 8 + 2 * w, 8u, epoch);
      BAR();
      STAMP(SB + 13);
      FRESH();
      const unsigned ab = OFF_X2 + (unsigned)(rt0 * 16) * 1024u + l16;
      v4u xf[32], sp[2];
      constexpr int AD = 12, FD = 6, NF = 96;
      sp[0] = ldb1(rs, OFF_ST + 2 * ST_BYTES + (unsigned)rt0 * 1024u + n * 64 + q * 16);
#pragma unroll
      for (int f = 0; f < AD; ++f) xf[f] = ldb1(rs, ab + f * 1024);
      f32x4 acc[6][2];
      Stats st;
      auto halfq = [&](auto HH, auto hook) {
        constexpr int H = HH;
        auto faddr = [&](int i) -> unsigned { return (unsigned)((QU + i % 6) * LU + (i / 6) * 1024); };
        v4u wf[FD];
#pragma unroll
        for (int i = 0; i < FD; ++i) wf[i] = ldsf(lb, faddr(i));
        SCHED();
        static_for<NF>([&](auto I) {
          constexpr int i = I, t = i % 6, ks = i / 6;
          const v4u f = wf[i % FD];
          if constexpr (i + FD < NF) wf[i % FD] = ldsf(lb, faddr(i + FD));
          if constexpr (t < 4) mfma1<ks == 0>(acc[t][H], xf[H * 16 + ks], f);
          else mfma1<ks == 0>(acc[t][H], f, xf[H * 16 + ks]);
          hook(I);
          SCHED();
        });
      };
      halfq(IC(0), [&](auto I) {
        constexpr int i = I;
        if constexpr (i % 12 == 2 && AD + i / 12 * 1 < 16) xf[AD + i / 12] = ldb1(rs, ab + (AD + i / 12) * 1024);
        if constexpr (i == 48) {
          poll_ge(fb, FC_X2 + g * 8 + 2 * w + 1, 8u, epoch);
          sp[1] = ldb1(rs, OFF_ST + 2 * ST_BYTES + (unsigned)(rt0 + 1) * 1024u + n * 64 + q * 16);
        }
        if constexpr (i > 48 && i <= 80 && (i & 1) == 0) xf[16 + (i - 50) / 2] = ldb1(rs, ab + (16 + (i - 50) / 2) * 1024);
      });
      halfq(IC(1), [&](auto I) {
        constexpr int i = I;
        if constexpr (i == 40) {
          make_stats(sp, st);
          if (q == 0) {
            *(LAS f32x2*)(L + L_STX + w * 256 + n * 8) = f32x2{st.a[0], st.b[0]};
            *(LAS f32x2*)(L + L_STX + w * 256 + 128 + n * 8) = f32x2{st.a[1], st.b[1]};
          }
        }
        if constexpr ((i & 7) == 4) {
          constexpr int k = i >> 3;
          const unsigned pc = (unsigned)((k & 3) * 4 + w) * 1024u;
          dma16(wsp + OFF_W1 + jo * 114688u + (k >> 2) * LU + pc, l16, lbase + W1U[1][k >> 2] * LU + pc);
        }
      });
      MFMA_FENCE();
#pragma unroll
      for (int t = 0; t < 6; ++t) { PINV(acc[t][0]); PINV(acc[t][1]); }
      f32x4 kva, kvb, za, zb;
      kv_epilogue(acc, L + L_STX + w * 256, (LAS float*)(L + L_CSQ), lane, kva, kvb, za, zb);
      kv_publish(L, L_KVX, rs, OFF_KVP + (unsigned)((16 + j) * 16 + g) * KVREC, lane, w, kva, kvb, za, zb);
      STAMP(SB + 14);
      publish(fb, FC_KV + (16 + j) * 2, tid);
      STAMP(SB + 15);
      dma_w123<64>(ws, OFF_WO + cs * 65536u, lbase + WOU[1] * LU, w, lane0);
      q_epilogue(acc, st, (LAS float*)(L + L_CSQ), lane, pq);
      if (b == 0 && tid == 0) __hip_atomic_store(fb + FO_EPOCH / 4, epoch, RLX_AGENT);
    }
  });
  STAMP_FLUSH(0, 64);
}

}

extern "C" void kernel_launch(void* const* d_in, const int* in_sizes, int n_in, void* d_out, int out_size, void* d_ws, size_t ws_size,
                              hipStream_t stream) {
  const u16* x = (const u16*)d_in[0];
  const u16* wqkv = (const u16*)d_in[1];
  const u16* wo = (const u16*)d_in[2];
  const u16* w1 = (const u16*)d_in[4];
  const u16* w2 = (const u16*)d_in[5];
  (void)hipFuncSetAttribute((const void*)kmain, hipFuncAttributeMaxDynamicSharedMemorySize, K2_LDS);
  kmain<<<256, 256, K2_LDS, stream>>>(x, wqkv, wo, w1, w2, (u16*)d_out, (unsigned char*)d_ws);
}
```

```cpp
#include <hip/hip_runtime.h>
#include <stdint.h>
#include <utility>

namespace {

typedef unsigned v4u __attribute__((ext_vector_type(4)));
typedef unsigned v2u __attribute__((ext_vector_type(2)));
typedef float f32x4 __attribute__((ext_vector_type(4)));
typedef float f32x2 __attribute__((ext_vector_type(2)));
typedef __bf16 bf2 __attribute__((ext_vector_type(2)));
typedef __bf16 bf8 __attribute__((ext_vector_type(8)));
typedef short s4v __attribute__((ext_vector_type(4)));
typedef unsigned short u16;
#define DI __device__ __forceinline__
#define GAS __attribute__((address_space(1)))
#define LAS __attribute__((address_space(3)))
typedef GAS unsigned gu32;
#define RLX_AGENT __ATOMIC_RELAXED, __HIP_MEMORY_SCOPE_AGENT
typedef __amdgpu_buffer_rsrc_t rsrc_t;

constexpr unsigned OFF_FLAGS = 0;
constexpr unsigned OFF_WQKV = 100352;
constexpr unsigned OFF_WO = OFF_WQKV + 1536u * 1024u;
constexpr unsigned OFF_W1 = OFF_WO + 512u * 1024u;
constexpr unsigned OFF_W2 = OFF_W1 + 1792u * 1024u;
constexpr unsigned OFF_CS1 = OFF_W2 + 1792u * 1024u;
constexpr unsigned OFF_CSQ = OFF_CS1 + 8u * 1792u * 4u;
constexpr unsigned OFF_PQ = OFF_CSQ + 16u * 96u * 4u;
constexpr unsigned KVREC = 2304;
constexpr unsigned OFF_KVP = OFF_PQ + 256u * 8192u;
constexpr unsigned OFF_ST = OFF_KVP + 2u * 256u * KVREC;
constexpr unsigned ST_BYTES = 128u * 1024u;
constexpr unsigned OFF_O = OFF_ST + 3u * ST_BYTES;
constexpr unsigned XF_BYTES = 128u * 16u * 1024u;
constexpr unsigned OFF_X1 = OFF_O + 2u * XF_BYTES;
constexpr unsigned OFF_X2 = OFF_X1 + 2u * XF_BYTES;
constexpr unsigned HF_BYTES = 128u * 56u * 1024u;
constexpr unsigned OFF_H = OFF_X2 + XF_BYTES;
constexpr unsigned WS_BYTES = OFF_H + 2u * HF_BYTES;
static_assert(OFF_O % 256 == 0 && OFF_KVP % 256 == 0 && OFF_PQ % 256 == 0 && OFF_ST % 256 == 0, "align");

constexpr unsigned FO_PRE = 0, FO_EPOCH = 1024, FO_TMO = 1088, FO_CNT = 2048;
constexpr int FC_O = 0;
constexpr int FC_X1 = 128;
constexpr int FC_H = 384;
constexpr int FC_X2 = 512;
constexpr int FC_KV = 640;
constexpr unsigned FLAG_BYTES = 2048 + 256 * 384;
static_assert(FO_CNT + (FC_KV + 64) * 128 <= FLAG_BYTES, "flags");
constexpr unsigned SPIN_CAP = 1u << 17;

constexpr int AUX_SC1 = 16;

constexpr unsigned LU = 16384;
constexpr unsigned L_SCR = 9 * LU;
constexpr unsigned L_CS1 = L_SCR;
constexpr unsigned L_CSQ = L_SCR + 512;
constexpr unsigned L_KVX = L_SCR + 1024;
constexpr unsigned L_CSP = L_SCR + 9728;
constexpr unsigned L_STX = L_SCR + 11264;
constexpr unsigned L_STAMP = L_SCR + 12288;
constexpr unsigned L_XB = 6 * LU;
constexpr int W1U[2][7] = {{4, 5, 6, 7, 8, 0, 1}, {0, 1, 8, 6, 7, 2, 3}};
constexpr int WOU[2] = {0, 2};
constexpr int QU = 2;
constexpr int REDU[2][3] = {{0, 1, 8}, {4, 5, 6}};
constexpr unsigned K2_LDS = 10 * LU;

DI f32x4 mfma(v4u a, v4u b, f32x4 c) {
  return __builtin_amdgcn_mfma_f32_16x16x32_bf16(__builtin_bit_cast(bf8, a), __builtin_bit_cast(bf8, b), c, 0, 0, 0);
}
template <bool FIRST> DI void mfma2(f32x4& c0, f32x4& c1, v4u a0, v4u b0, v4u a1, v4u b1) {
  if constexpr (FIRST)
    asm volatile("v_mfma_f32_16x16x32_bf16 %0, %2, %3, 0\n\tv_mfma_f32_16x16x32_bf16 %1, %4, %5, 0"
                 : "=&v"(c0), "=&v"(c1) : "v"(a0), "v"(b0), "v"(a1), "v"(b1));
  else
    asm volatile("v_mfma_f32_16x16x32_bf16 %0, %2, %3, %0\n\tv_mfma_f32_16x16x32_bf16 %1, %4, %5, %1"
                 : "+v"(c0), "+v"(c1) : "v"(a0), "v"(b0), "v"(a1), "v"(b1));
}
template <bool FIRST> DI void mfma1(f32x4& c0, v4u a0, v4u b0) {
  if constexpr (FIRST) asm volatile("v_mfma_f32_16x16x32_bf16 %0, %1, %2, 0" : "=&v"(c0) : "v"(a0), "v"(b0));
  else asm volatile("v_mfma_f32_16x16x32_bf16 %0, %1, %2, %0" : "+v"(c0) : "v"(a0), "v"(b0));
}
template <bool FIRST> DI void mfma4a(f32x4 (&c)[4], v4u w0, v4u w1, v4u w2, v4u w3, v4u b) {
  if constexpr (FIRST)
    asm volatile("s_nop 1\n\tv_mfma_f32_16x16x32_bf16 %0, %4, %8, 0\n\tv_mfma_f32_16x16x32_bf16 %1, %5, %8, 0\n\t"
                 "v_mfma_f32_16x16x32_bf16 %2, %6, %8, 0\n\tv_mfma_f32_16x16x32_bf16 %3, %7, %8, 0"
                 : "=&v"(c[0]), "=&v"(c[1]), "=&v"(c[2]), "=&v"(c[3]) : "a"(w0), "a"(w1), "a"(w2), "a"(w3), "v"(b));
  else
    asm volatile("s_nop 1\n\tv_mfma_f32_16x16x32_bf16 %0, %4, %8, %0\n\tv_mfma_f32_16x16x32_bf16 %1, %5, %8, %1\n\t"
                 "v_mfma_f32_16x16x32_bf16 %2, %6, %8, %2\n\tv_mfma_f32_16x16x32_bf16 %3, %7, %8, %3"
                 : "+v"(c[0]), "+v"(c[1]), "+v"(c[2]), "+v"(c[3]) : "a"(w0), "a"(w1), "a"(w2), "a"(w3), "v"(b));
}
#define MFMA_FENCE() asm volatile("s_nop 15\n\ts_nop 3" ::: "memory")
DI void pinv(f32x4& x) { asm volatile("" : "+v"(x)); }
#define PINV(x) pinv(x)
DI unsigned pack2(float a, float b) {
  f32x2 v = {a, b};
  return __builtin_bit_cast(unsigned, __builtin_convertvector(v, bf2));
}
DI float bflo(unsigned u) { return __builtin_bit_cast(float, u << 16); }
DI float bfhi(unsigned u) { return __builtin_bit_cast(float, u & 0xffff0000u); }
DI float dot2(unsigned a, unsigned b, float c) {
  return __builtin_amdgcn_fdot2_f32_bf16(__builtin_bit_cast(bf2, a), __builtin_bit_cast(bf2, b), c, false);
}
constexpr float LOG2E = 1.4426950408889634f;
DI float phi(float t) { return __builtin_amdgcn_exp2f(fminf(t, 0.f) * LOG2E) + (fmaxf(t, 0.f) + 1e-6f); }
DI float silu(float t) { return t * __builtin_amdgcn_rcpf(1.0f + __builtin_amdgcn_exp2f(t * -LOG2E)); }
DI v4u ldb(rsrc_t rs, unsigned off) { return __builtin_amdgcn_raw_buffer_load_b128(rs, off, 0, 0); }
DI v4u ldb1(rsrc_t rs, unsigned off) { return __builtin_amdgcn_raw_buffer_load_b128(rs, off, 0, AUX_SC1); }
DI v4u lda128(v4u srd, unsigned off) {
  v4u t;
  asm volatile("buffer_load_dwordx4 %0, %1, %2, 0 offen sc1" : "=a"(t) : "v"(off), "s"(srd) : "memory");
  return t;
}
DI void stb1(rsrc_t rs, unsigned off, v4u v) { __builtin_amdgcn_raw_buffer_store_b128(v, rs, off, 0, AUX_SC1); }
DI void stb1_64(rsrc_t rs, unsigned off, v2u v) { __builtin_amdgcn_raw_buffer_store_b64(v, rs, off, 0, AUX_SC1); }
#define DRAIN() asm volatile("s_waitcnt vmcnt(0)" ::: "memory")
#define LGKM0() asm volatile("s_waitcnt lgkmcnt(0)" ::: "memory")
#define BAR() do { asm volatile("s_waitcnt lgkmcnt(0)" ::: "memory"); __builtin_amdgcn_s_barrier(); asm volatile("" ::: "memory"); } while (0)
#define SCHED() __builtin_amdgcn_sched_barrier(0)
#define PIN(x) asm volatile("" : "+v"(x))
#define PIN4(a, o) asm volatile("" : "+v"(a[(o)]), "+v"(a[(o) + 1]), "+v"(a[(o) + 2]), "+v"(a[(o) + 3]))
#define STAMP(i) do {} while (0)
#define STAMP_INIT() do {} while (0)
#define STAMP_FLUSH(lo, hi) do {} while (0)

template <int... I, typename F> DI void static_for_impl(std::integer_sequence<int, I...>, F&& f) { (f(std::integral_constant<int, I>{}), ...); }
template <int N, typename F> DI void static_for(F&& f) { static_for_impl(std::make_integer_sequence<int, N>{}, f); }

DI void dma16(const void* sbase, unsigned voff, unsigned lds_dst) {
  unsigned keep;
  asm volatile("s_mov_b32 %0, m0\n\ts_mov_b32 m0, %3\n\ts_nop 3\n\tglobal_load_lds_dwordx4 %1, %2 sc1\n\ts_mov_b32 m0, %0"
               : "=&s"(keep) : "v"(voff), "s"(sbase), "s"(lds_dst) : "memory");
}
DI void dma16_in(const void* sbase, unsigned voff, unsigned lds_dst) {
  unsigned keep;
  asm volatile("s_mov_b32 %0, m0\n\ts_mov_b32 m0, %3\n\ts_nop 3\n\tglobal_load_lds_dwordx4 %1, %2\n\ts_mov_b32 m0, %0"
               : "=&s"(keep) : "v"(voff), "s"(sbase), "s"(lds_dst) : "memory");
}
DI v4u tr_frag(LAS unsigned char* p) {
  s4v lo = __builtin_amdgcn_ds_read_tr16_b64_v4i16((LAS s4v*)(p));
  s4v hi = __builtin_amdgcn_ds_read_tr16_b64_v4i16((LAS s4v*)(p + 256));
  v2u l2 = __builtin_bit_cast(v2u, lo), h2 = __builtin_bit_cast(v2u, hi);
  return v4u{l2.x, l2.y, h2.x, h2.y};
}
DI float sum16(float v) {
  const unsigned u = __builtin_bit_cast(unsigned, v);
  unsigned u2 = u; PIN(u2);
  auto r = __builtin_amdgcn_permlane16_swap(u, u2, false, false);
  const unsigned a = r[0], b = r[1];
  return __builtin_bit_cast(float, a) + __builtin_bit_cast(float, b);
}
DI float sum32(float v) {
  const unsigned u = __builtin_bit_cast(unsigned, v);
  unsigned u2 = u; PIN(u2);
  auto r = __builtin_amdgcn_permlane32_swap(u, u2, false, false);
  const unsigned a = r[0], b = r[1];
  return __builtin_bit_cast(float, a) + __builtin_bit_cast(float, b);
}
DI float sum_q(float v) { return sum32(sum16(v)); }
DI v4u pair16(unsigned a0, unsigned a1, unsigned b0, unsigned b1) {
  auto r0 = __builtin_amdgcn_permlane16_swap(a0, b0, false, false);
  auto r1 = __builtin_amdgcn_permlane16_swap(a1, b1, false, false);
  const unsigned x0 = r0[0], x1 = r1[0], x2 = r0[1], x3 = r1[1];
  return v4u{x0, x1, x2, x3};
}

DI bool spin_fail(unsigned& sp, gu32* fb, unsigned epoch) {
  if (++sp > SPIN_CAP) { __hip_atomic_store(fb + FO_TMO / 4, epoch, RLX_AGENT); return true; }
  return (sp & 255u) == 0u && __hip_atomic_load(fb + FO_TMO / 4, RLX_AGENT) == epoch;
}
DI void poll_ge(gu32* fb, int line, unsigned want, unsigned epoch) {
  gu32* wd = fb + (FO_CNT / 4) + line * 32;
  for (unsigned sp = 0;;) {
    if ((unsigned)__builtin_amdgcn_readfirstlane(__hip_atomic_load(wd, RLX_AGENT)) >= want) return;
    __builtin_amdgcn_s_sleep(1);
    if (spin_fail(sp, fb, epoch)) return;
  }
}
DI void poll_ge2(gu32* fb, int line, unsigned want, unsigned epoch, int lane) {
  gu32* wd = fb + (FO_CNT / 4) + line * 32 + (lane & 1);
  for (unsigned sp = 0;;) {
    if (__all(__hip_atomic_load(wd, RLX_AGENT) >= want)) return;
    __builtin_amdgcn_s_sleep(1);
    if (spin_fail(sp, fb, epoch)) return;
  }
}
DI void arrive(gu32* fb, int line, int dw, int lane) { if (lane == 0) __hip_atomic_fetch_add(fb + (FO_CNT / 4) + line * 32 + dw, 1u, RLX_AGENT); }

struct Stats { float a[2], b[2]; };
DI void finish_stats(float s1, float s2, float& a, float& b) {
  const float mean = s1 * (1.0f / 512.0f);
  const float var = fmaxf(s2 * (1.0f / 512.0f) - mean * mean, 0.f);
  a = __builtin_amdgcn_rsqf(var + 1e-5f);
  b = -mean * a;
}

DI void kv_epilogue(const f32x4 (&acc)[6][2], LAS unsigned char* stx, LAS float* csq, int lane, f32x4& kva, f32x4& kvb, f32x4& za, f32x4& zb) {
  const int q = lane >> 4, n = lane & 15;
  f32x4 ab[2][2];
#pragma unroll
  for (int rt = 0; rt < 2; ++rt) { ab[rt][0] = *(LAS f32x4*)(stx + rt * 128 + q * 32); ab[rt][1] = *(LAS f32x4*)(stx + rt * 128 + q * 32 + 16); }
  v4u PK[2], V[2];
#pragma unroll
  for (int h = 0; h < 2; ++h) {
    const float ck = csq[(2 * h) * 16 + n], cv = csq[(2 * h + 1) * 16 + n];
    unsigned pk[4], pv[4];
#pragma unroll
    for (int rt = 0; rt < 2; ++rt) {
      float a[4], b[4];
#pragma unroll
      for (int r = 0; r < 4; ++r) {
        const float sa = ab[rt][r >> 1][(r & 1) * 2], sb = ab[rt][r >> 1][(r & 1) * 2 + 1];
        a[r] = phi(fmaf(acc[2 * h][rt][r], sa, sb * ck));
        b[r] = fmaf(acc[2 * h + 1][rt][r], sa, sb * cv);
      }
      pk[2 * rt] = pack2(a[0], a[1]); pk[2 * rt + 1] = pack2(a[2], a[3]);
      pv[2 * rt] = pack2(b[0], b[1]); pv[2 * rt + 1] = pack2(b[2], b[3]);
    }
    PK[h] = v4u{pk[0], pk[1], pk[2], pk[3]};
    V[h] = v4u{pv[0], pv[1], pv[2], pv[3]};
  }
  const f32x4 z4 = {0, 0, 0, 0};
  const v4u ones = {0x3f803f80u, 0x3f803f80u, 0x3f803f80u, 0x3f803f80u};
  kva = mfma(PK[0], V[0], z4); kvb = mfma(PK[1], V[1], z4);
  za = mfma(PK[0], ones, z4); zb = mfma(PK[1], ones, z4);
}
DI void q_epilogue(const f32x4 (&acc)[6][2], const Stats& st, LAS float* csq, int lane, v4u (&pq)[2]) {
  const int q = lane >> 4;
#pragma unroll
  for (int rt = 0; rt < 2; ++rt) {
    unsigned p[4];
#pragma unroll
    for (int t = 0; t < 2; ++t) {
      const f32x4 c = *(LAS f32x4*)(csq + (4 + t) * 16 + 4 * q);
      float v[4];
#pragma unroll
      for (int r = 0; r < 4; ++r) v[r] = phi(fmaf(acc[4 + t][rt][r], st.a[rt], st.b[rt] * c[r]));
      p[2 * t] = pack2(v[0], v[1]); p[2 * t + 1] = pack2(v[2], v[3]);
    }
    pq[rt] = v4u{p[0], p[1], p[2], p[3]};
  }
}

DI void kv_publish(LAS unsigned char* L, unsigned kvx, rsrc_t rs, unsigned rec_off, int lane, int w, f32x4 kva, f32x4 kvb, f32x4 za, f32x4 zb) {
  LAS unsigned char* me = L + kvx + w * 2176;
  *(LAS f32x4*)(me + lane * 16) = kva;
  *(LAS f32x4*)(me + 1024 + lane * 16) = kvb;
  if ((lane & 15) == 0) {
    *(LAS f32x4*)(me + 2048 + (lane >> 4) * 16) = za;
    *(LAS f32x4*)(me + 2112 + (lane >> 4) * 16) = zb;
  }
  BAR();
  if (w < 2 || (w == 2 && lane < 8)) {
    const unsigned o = w * 1024 + lane * 16;
    f32x4 s = *(LAS f32x4*)(L + kvx + o);
#pragma unroll
    for (int ww = 1; ww < 4; ++ww) s += *(LAS f32x4*)(L + kvx + ww * 2176 + o);
    stb1(rs, rec_off + o, __builtin_bit_cast(v4u, s));
  }
}

struct LBase { LAS unsigned char* b[3]; };
DI v4u ldsf(const LBase& lb, unsigned off) { return *(LAS v4u*)(lb.b[off >> 16] + (off & 0xffffu)); }

DI void publish(gu32* fb, int line, int tid) {
  DRAIN();
  BAR();
  arrive(fb, line, 0, tid);
}
template <int NKB>
DI void dma_block(const unsigned char* ws, unsigned src, unsigned ldst, int w, int lane) {
  static_assert(NKB % 16 == 0, "");
  const unsigned char* p = ws + src + (unsigned)w * (NKB / 4) * 1024u;
  unsigned d = ldst + (unsigned)w * (NKB / 4) * 1024u;
#pragma unroll 1
  for (int i = 0; i < NKB / 16; ++i) {
    dma16(p, (unsigned)lane * 16u, d);
    dma16(p + 1024, (unsigned)lane * 16u, d + 1024);
    dma16(p + 2048, (unsigned)lane * 16u, d + 2048);
    dma16(p + 3072, (unsigned)lane * 16u, d + 3072);
    p += 4096; d += 4096;
  }
}
template <int NKB>
DI void dma_w123(const unsigned char* ws, unsigned src, unsigned ldst, int w, int lane) {
  if (w == 0) return;
  const unsigned char* p = ws + src + (unsigned)(w - 1) * 1024u;
  unsigned d = ldst + (unsigned)(w - 1) * 1024u;
#pragma unroll 1
  for (int i = w - 1; i < NKB; i += 3) {
    dma16(p, (unsigned)lane * 16u, d);
    p += 3072; d += 3072;
  }
}
template <int NT, int NTN, int AD, int FD, typename FA, typename HOOK>
DI void gemm_k(rsrc_t rs, unsigned ab, const LBase& lb, f32x4 (&acc)[NT][2], FA faddr, HOOK hook) {
  constexpr int R = AD + 1, NF = 16 * NT;
  v4u xa[R], xb[R];
#pragma unroll
  for (int ks = 0; ks < AD; ++ks) { xa[ks] = ldb1(rs, ab + ks * 1024); xb[ks] = ldb1(rs, ab + (16 + ks) * 1024); }
  v4u wf[FD];
#pragma unroll
  for (int i = 0; i < FD; ++i) wf[i] = ldsf(lb, faddr(i % NT, i / NT));
  SCHED();
  static_for<NF>([&](auto I) {
    constexpr int i = I, t = i % NT, ks = i / NT;
    const v4u f = wf[i % FD];
    if constexpr (i + FD < NF) wf[i % FD] = ldsf(lb, faddr((i + FD) % NT, (i + FD) / NT));
    if constexpr (t < NTN) mfma2<ks == 0>(acc[t][0], acc[t][1], xa[ks % R], f, xb[ks % R], f);
    else mfma2<ks == 0>(acc[t][0], acc[t][1], f, xa[ks % R], f, xb[ks % R]);
    if constexpr (t == 1 && ks + AD < 16) { xa[(ks + AD) % R] = ldb1(rs, ab + (ks + AD) * 1024); xb[(ks + AD) % R] = ldb1(rs, ab + (16 + ks + AD) * 1024); }
    hook(I);
    SCHED();
  });
  MFMA_FENCE();
#pragma unroll
  for (int t = 0; t < NT; ++t) { PINV(acc[t][0]); PINV(acc[t][1]); }
}
DI void res_update(v2u (&res)[4], const f32x4 (&acc)[4]) {
#pragma unroll
  for (int c = 0; c < 4; ++c) {
    const unsigned t0 = pack2(acc[c][0], acc[c][1]), t1 = pack2(acc[c][2], acc[c][3]);
    res[c].x = pack2(bflo(res[c].x) + bflo(t0), bfhi(res[c].x) + bfhi(t0));
    res[c].y = pack2(bflo(res[c].y) + bflo(t1), bfhi(res[c].y) + bfhi(t1));
  }
}
DI void store_x_tile(rsrc_t rs, unsigned xoff, unsigned stoff, const v2u (&res)[4], int myrt, int cs, int lane) {
  const int q = lane >> 4, n = lane & 15;
  const unsigned kg = (q & 1) * 2 + (q >> 1);
#pragma unroll
  for (int c2 = 0; c2 < 2; ++c2) {
    const v4u o = pair16(res[2 * c2].x, res[2 * c2].y, res[2 * c2 + 1].x, res[2 * c2 + 1].y);
    stb1(rs, xoff + (unsigned)(myrt * 16 + 2 * cs + c2) * 1024u + (kg * 16 + n) * 16, o);
  }
  float s1 = 0, s2 = 0;
#pragma unroll
  for (int c = 0; c < 4; ++c) {
    s1 = dot2(res[c].x, 0x3f803f80u, s1); s2 = dot2(res[c].x, res[c].x, s2);
    s1 = dot2(res[c].y, 0x3f803f80u, s1); s2 = dot2(res[c].y, res[c].y, s2);
  }
  s1 = sum_q(s1); s2 = sum_q(s2);
  if (q == 0) stb1_64(rs, stoff + (unsigned)myrt * 1024u + n * 64 + cs * 8, __builtin_bit_cast(v2u, f32x2{s1, s2}));
}
DI void load_stats(rsrc_t rs, unsigned stoff, int rt0, int lane, v4u (&sp)[2]) {
  const int q = lane >> 4, n = lane & 15;
#pragma unroll
  for (int rt = 0; rt < 2; ++rt) sp[rt] = ldb1(rs, stoff + (unsigned)(rt0 + rt) * 1024u + n * 64 + q * 16);
}
DI void make_stats(const v4u (&sp)[2], Stats& st) {
#pragma unroll
  for (int rt = 0; rt < 2; ++rt) {
    const f32x4 p = __builtin_bit_cast(f32x4, sp[rt]);
    finish_stats(sum_q(p[0] + p[2]), sum_q(p[1] + p[3]), st.a[rt], st.b[rt]);
  }
}

__global__ void __launch_bounds__(256, 1) kmain(const u16* __restrict__ x, const u16* __restrict__ wqkv, const u16* __restrict__ wo,
                                                const u16* __restrict__ w1, const u16* __restrict__ w2g, u16* __restrict__ out,
                                                unsigned char* __restrict__ ws) {
  extern __shared__ __attribute__((aligned(16))) unsigned char smem[];
  LAS unsigned char* L = (LAS unsigned char*)smem;
  constexpr unsigned LSTAMP = L_STAMP;
  const unsigned lbase = (unsigned)(size_t)L;
  const int tid = threadIdx.x, lane0 = tid & 63, w = __builtin_amdgcn_readfirstlane(tid >> 6);
  const int b = blockIdx.x;
  const rsrc_t rs = __builtin_amdgcn_make_buffer_rsrc((void*)ws, 0, WS_BYTES, 0x00020000);
  const v4u srd = {(unsigned)(size_t)ws, (unsigned)((size_t)ws >> 32) & 0xffffu, WS_BYTES, 0x00020000u};
  gu32* fb = (gu32*)(ws + OFF_FLAGS);
  const f32x4 z4 = {0, 0, 0, 0};
  const int gm = 4 * ((b & 7) >> 1) + (b >> 6), jm = 8 * (b & 1) + ((b >> 3) & 7);
  const int hh = (jm >> 2) & 1, cs = (jm & 3) | ((jm >> 3) << 2);
#define FRESH() int lane = lane0; PIN(lane); const int q = lane >> 4, n = lane & 15; const unsigned l16 = (unsigned)lane * 16u; \
  LBase lb; { unsigned o1 = l16 + 65536u, o2 = l16 + 131072u; PIN(o1); PIN(o2); lb.b[0] = L + l16; lb.b[1] = L + o1; lb.b[2] = L + o2; } (void)q; (void)n; (void)lb; \
  const unsigned char* wsp = ws; asm volatile("" : "+s"(wsp)); int jo = jm; asm volatile("" : "+s"(jo)); (void)wsp; (void)jo;
  STAMP_INIT();
  STAMP(0);
  const unsigned epoch = __hip_atomic_load(fb + FO_EPOCH / 4, RLX_AGENT) + 1u;

  v4u w2[14][4];
  auto ldw2 = [&](auto I, unsigned l16) {
    constexpr int i = I;
    w2[i >> 2][i & 3] = ldb1(rs, OFF_W2 + (unsigned)((cs * 4 + w) * 56 + i) * 1024u + l16);
  };
  {
    FRESH();
    const int g = 4 * ((b & 7) >> 1) + (b >> 6), j = 8 * (b & 1) + ((b >> 3) & 7);
    const int drow = lane >> 2, dch = (lane & 3) ^ (((drow >> 3) & 1) << 1);
    {
      const unsigned voff = (unsigned)(drow * 1536 + 32 * j + dch * 8) * 2u;
#pragma unroll
      for (int i = 0; i < 24; ++i) {
        const int it = 4 * i + w, rb = it / 3, seg = it % 3;
        dma16_in((const unsigned char*)wqkv + (size_t)(rb * 16 * 1536 + seg * 512) * 2, voff, lbase + (seg * 32 + rb) * 1024);
      }
    }
    v4u xr[4][8];
    {
      const GAS v4u* xp = (const GAS v4u*)(x + (size_t)(128 * g + 32 * w + (lane >> 5)) * 512 + (lane & 31) * 8);
#pragma unroll
      for (int blk = 0; blk < 4; ++blk)
#pragma unroll
        for (int i = 0; i < 8; ++i) xr[blk][i] = xp[((blk >> 1) * 16 + 2 * i) * 64 + (blk & 1) * 32];
    }
    const u16* psrc; int pld, pp, pks, pkind, prow[4];
    if (b < 112) { psrc = w1; pld = 1792; pp = b >> 1; pks = 8 * (b & 1) + 2 * w; pkind = 0; }
    else {
      const int u = (b - 112) * 8 + 2 * w;
      if (u < 896) { psrc = w2g; pld = 512; pp = u / 56; pks = u % 56; pkind = 1; }
      else { psrc = wo; pld = 512; pp = (u - 896) >> 4; pks = (u - 896) & 15; pkind = 2; }
    }
#pragma unroll
    for (int i = 0; i < 4; ++i) {
      const int ks = pks + (i >> 1);
      prow[i] = pkind == 1 ? 112 * (2 * (ks / 7) + (i & 1)) + 16 * (ks % 7) : 32 * ks + 16 * (i & 1);
    }
    v4u pt[4];
#pragma unroll
    for (int i = 0; i < 4; ++i) pt[i] = *(const GAS v4u*)(psrc + (size_t)(prow[i] + drow) * pld + 32 * pp + dch * 8);
    SCHED();
    if (tid < 24) stb1(rs, OFF_FLAGS + FO_CNT + b * 384 + tid * 16, v4u{0u, 0u, 0u, 0u});
    STAMP(1);

    v4u xa[16], xb[16];
    float s1a = 0, s2a = 0, s1b = 0, s2b = 0;
    LAS unsigned char* B = L + L_XB + w * 8192;
    {
      const int c = lane & 31, ksl = c >> 2, kgw = c & 3, fw = (ksl & 1) * 4 + kgw;
      LAS unsigned char* wp = B + ksl * 1024 + kgw * 256;
      LAS unsigned char* rp0 = B + q * 256 + ((n ^ q) * 16);
      LAS unsigned char* rp1 = B + q * 256 + ((n ^ (4 + q)) * 16);
#pragma unroll
      for (int blk = 0; blk < 4; ++blk) {
#pragma unroll
        for (int i = 0; i < 8; ++i) *(LAS v4u*)(wp + (((2 * i + (lane >> 5)) ^ fw) * 16)) = xr[blk][i];
        LGKM0();
#pragma unroll
        for (int k8 = 0; k8 < 8; ++k8) {
          const v4u f = *(LAS v4u*)(((k8 & 1) ? rp1 : rp0) + k8 * 1024);
          if (blk < 2) xa[(blk & 1) * 8 + k8] = f; else xb[(blk & 1) * 8 + k8] = f;
#pragma unroll
          for (int e = 0; e < 4; ++e) {
            if (blk < 2) { s1a = dot2(f[e], 0x3f803f80u, s1a); s2a = dot2(f[e], f[e], s2a); }
            else { s1b = dot2(f[e], 0x3f803f80u, s1b); s2b = dot2(f[e], f[e], s2b); }
          }
        }
        PIN(s1a); PIN(s2a); PIN(s1b); PIN(s2b);
        LGKM0();
      }
    }
    STAMP(2);
    const int r4 = n >> 2, c4 = n & 3;
    unsigned trb[2];
#pragma unroll
    for (int ct = 0; ct < 2; ++ct)
      trb[ct] = (unsigned)((q >> 1) * 1024 + ((q & 1) * 8 + r4) * 64 + (2 * (ct ^ (q & 1)) + (c4 >> 1)) * 16 + (c4 & 1) * 8);
    const v4u ones = {0x3f803f80u, 0x3f803f80u, 0x3f803f80u, 0x3f803f80u};
    {
#pragma unroll
      for (int i = 0; i < 4; ++i) *(LAS v4u*)(B + i * 1024 + l16) = pt[i];
      LGKM0();
      f32x4 c1[2] = {z4, z4};
#pragma unroll
      for (int u = 0; u < 2; ++u)
#pragma unroll
        for (int ct = 0; ct < 2; ++ct) {
          const v4u f = tr_frag(B + u * 2048 + trb[ct]);
          const int gct = 2 * pp + ct, ks = pks + u;
          unsigned dst;
          if (pkind == 0) dst = OFF_W1 + (unsigned)((gct / 7) * 112 + (gct % 7) * 16 + ks) * 1024u;
          else if (pkind == 1) { const int p = ks / 7, t = ks % 7; dst = OFF_W2 + (unsigned)(((gct >> 2) * 4 + (p >> 1)) * 56 + (2 * t + (p & 1)) * 4 + (gct & 3)) * 1024u; }
          else dst = OFF_WO + (unsigned)((gct >> 2) * 64 + (gct & 3) * 16 + ks) * 1024u;
          stb1(rs, dst + l16, f);
          c1[ct] = mfma(f, ones, c1[ct]);
        }
      if (pkind == 0 && n == 0) {
        const int part = (b & 1) * 4 + w;
#pragma unroll
        for (int ct = 0; ct < 2; ++ct)
          stb1(rs, OFF_CS1 + (unsigned)(part * 1792 + 32 * pp + 16 * ct + 4 * q) * 4u, __builtin_bit_cast(v4u, c1[ct]));
      }
    }
    Stats st;
    finish_stats(sum_q(s1a), sum_q(s2a), st.a[0], st.b[0]);
    finish_stats(sum_q(s1b), sum_q(s2b), st.a[1], st.b[1]);
    if (q == 0) {
      *(LAS f32x2*)(L + L_STX + w * 256 + n * 8) = f32x2{st.a[0], st.b[0]};
      *(LAS f32x2*)(L + L_STX + w * 256 + 128 + n * 8) = f32x2{st.a[1], st.b[1]};
    }
    STAMP(3);
    DRAIN();
    BAR();
    STAMP(4);
    {
      f32x4 cs[6] = {z4, z4, z4, z4, z4, z4};
#pragma unroll
      for (int i = 0; i < 4; ++i)
#pragma unroll
        for (int seg = 0; seg < 3; ++seg) {
          LAS unsigned char* blk = L + seg * 32768 + (4 * w + i) * 2048;
          const v4u f0 = tr_frag(blk + trb[0]), f1 = tr_frag(blk + trb[1]);
          const int t0 = seg == 0 ? 4 : seg - 1, t1 = seg == 0 ? 5 : seg + 1;
          if (seg == 0) { cs[4] = mfma(f0, ones, cs[4]); cs[5] = mfma(f1, ones, cs[5]); }
          else { cs[seg - 1] = mfma(ones, f0, cs[seg - 1]); cs[seg + 1] = mfma(ones, f1, cs[seg + 1]); }
          *(LAS v4u*)(blk + l16) = f0;
          *(LAS v4u*)(blk + 1024 + l16) = f1;
          if (4 * w + i == g) {
            stb1(rs, OFF_WQKV + (unsigned)(j * 96 + t0 * 16 + g) * 1024u + l16, f0);
            stb1(rs, OFF_WQKV + (unsigned)(j * 96 + t1 * 16 + g) * 1024u + l16, f1);
          }
        }
      LAS float* csw = (LAS float*)(L + L_CSP) + w * 96;
#pragma unroll
      for (int t = 4; t < 6; ++t) if (n == 0) *(LAS f32x4*)(csw + t * 16 + 4 * q) = cs[t];
#pragma unroll
      for (int t = 0; t < 4; ++t) if (q == 0) csw[t * 16 + n] = cs[t][0];
    }
    BAR();
    if (tid < 96) {
      LAS float* c = (LAS float*)(L + L_CSP);
      const float s = (c[tid] + c[96 + tid]) + (c[192 + tid] + c[288 + tid]);
      ((LAS float*)(L + L_CSQ))[tid] = s;
      if (g == 0) __hip_atomic_store((GAS float*)(ws + OFF_CSQ) + j * 96 + tid, s, RLX_AGENT);
    }
    DRAIN();
    BAR();
    if (tid == 0) __hip_atomic_store(fb + FO_PRE / 4 + b, epoch, RLX_AGENT);
    f32x4 acc[6][2];
    {
      constexpr int FD = 6, NF = 96;
      auto addr = [&](int i) -> unsigned {
        const int t = i % 6, ks = i / 6;
        const int seg = t < 4 ? 1 + (t & 1) : 0, ct = t < 4 ? (t >> 1) : (t - 4);
        return (unsigned)(seg * 32768 + ks * 2048 + ct * 1024);
      };
      v4u wf[FD];
#pragma unroll
      for (int i = 0; i < FD; ++i) wf[i] = ldsf(lb, addr(i));
      SCHED();
      static_for<NF>([&](auto I) {
        constexpr int i = I, t = i % 6, ks = i / 6;
        const v4u f = wf[i % FD];
        if constexpr (i + FD < NF) wf[i % FD] = ldsf(lb, addr(i + FD));
        if constexpr (t < 4) mfma2<ks == 0>(acc[t][0], acc[t][1], xa[ks], f, xb[ks], f);
        else mfma2<ks == 0>(acc[t][0], acc[t][1], f, xa[ks], f, xb[ks]);
        SCHED();
      });
    }
    MFMA_FENCE();
#pragma unroll
    for (int t = 0; t < 6; ++t) { PINV(acc[t][0]); PINV(acc[t][1]); }
    STAMP(5);
    f32x4 kva, kvb, za, zb;
    kv_epilogue(acc, L + L_STX + w * 256, (LAS float*)(L + L_CSQ), lane, kva, kvb, za, zb);
    v4u pq0[2];
    q_epilogue(acc, st, (LAS float*)(L + L_CSQ), lane, pq0);
    const int cu = g * 16 + j;
#pragma unroll
    for (int rt = 0; rt < 2; ++rt) stb1(rs, OFF_PQ + (unsigned)((cu * 4 + w) * 2 + rt) * 1024u + l16, pq0[rt]);
    if (w == 0) {
      for (unsigned sp = 0;;) {
        const v4u v = ldb1(rs, OFF_FLAGS + FO_PRE + l16);
        if (__all(v.x == epoch && v.y == epoch && v.z == epoch && v.w == epoch)) break;
        __builtin_amdgcn_s_sleep(4);
        if (spin_fail(sp, fb, epoch)) break;
      }
    }
    kv_publish(L, L_KVX, rs, OFF_KVP + (unsigned)(j * 16 + g) * KVREC, lane, w, kva, kvb, za, zb);
    STAMP(6);
    publish(fb, FC_KV + 2 * j, tid);
    STAMP(7);
  }

  const int g = gm, j = jm;
  const int cu = g * 16 + j;
  const int myrt = 8 * g + 4 * hh + w;
  const int rt0 = 8 * g + 2 * w;
  v4u pq[2];
  v2u res[4];
  dma_w123<64>(ws, OFF_WO + cs * 65536u, lbase + WOU[0] * LU, w, lane0);
  dma_w123<32>(ws, OFF_W1 + j * 114688u, lbase + W1U[0][0] * LU, w, lane0);
  static_for<14>([&](auto I) { ldw2(I, (unsigned)lane0 * 16u); });
  {
    FRESH();
    const GAS v2u* xp = (const GAS v2u*)(x + (size_t)(16 * myrt + n) * 512 + 64 * cs + 4 * q);
#pragma unroll
    for (int c = 0; c < 4; ++c) res[c] = xp[c * 4];
    if (tid < 112) {
      float csv = 0.f;
#pragma unroll
      for (int i = 0; i < 8; ++i) csv += __hip_atomic_load((GAS float*)(ws + OFF_CS1) + i * 1792 + 112 * j + tid, RLX_AGENT);
      ((LAS float*)(L + L_CS1))[tid] = csv;
    } else if (tid >= 128 && tid < 224) {
      ((LAS float*)(L + L_CSQ))[tid - 128] = __hip_atomic_load((GAS float*)(ws + OFF_CSQ) + j * 96 + tid - 128, RLX_AGENT);
    }
  }
  using std::integral_constant;
#define IC(x) integral_constant<int, (x)>{}
#define VMW(n) asm volatile("s_waitcnt vmcnt(%0)" :: "i"(n) : "memory")

  static_for<2>([&](auto LL) {
    constexpr int l = LL;
    constexpr int SB = 10 + l * 24;
    if (w == 0) poll_ge(fb, FC_KV + (l * 16 + j) * 2, 16u, epoch);
    BAR();
    STAMP(SB + 0);
    {
      FRESH();
      if constexpr (l == 0) {
#pragma unroll
        for (int rt = 0; rt < 2; ++rt) pq[rt] = ldb1(rs, OFF_PQ + (unsigned)((cu * 4 + w) * 2 + rt) * 1024u + l16);
      }
      const unsigned rb = OFF_KVP + (unsigned)((l * 16 + j) * 16) * KVREC;
      if (w < 3) {
        const unsigned o = rb + (w < 2 ? (unsigned)w * 1024u + l16 : 2048u + (unsigned)(lane & 7) * 16u);
        v4u a[16];
#pragma unroll
        for (int i = 0; i < 16; ++i) a[i] = ldb1(rs, o + i * KVREC);
        SCHED();
        f32x4 s[4];
#pragma unroll
        for (int i = 0; i < 4; ++i)
          s[i] = (__builtin_bit_cast(f32x4, a[4 * i]) + __builtin_bit_cast(f32x4, a[4 * i + 1])) + (__builtin_bit_cast(f32x4, a[4 * i + 2]) + __builtin_bit_cast(f32x4, a[4 * i + 3]));
        const f32x4 t = (s[0] + s[1]) + (s[2] + s[3]);
        const v2u pk = v2u{pack2(t[0], t[1]), pack2(t[2], t[3])};
        if (w < 2) *(LAS v2u*)(L + L_KVX + w * 512 + lane * 8) = pk;
        else if (lane < 8) *(LAS v2u*)(L + L_KVX + 1024 + lane * 8) = pk;
      }
      BAR();
      const v2u ka = *(LAS v2u*)(L + L_KVX + lane * 8), kb = *(LAS v2u*)(L + L_KVX + 512 + lane * 8);
      const v2u zz = *(LAS v2u*)(L + L_KVX + 1024 + ((n & 1) * 4 + q) * 8);
      const v4u Aa = {ka.x, ka.y, 0u, 0u};
      const v4u Ab = {0u, 0u, kb.x, kb.y};
      const bool za_ = (n & 3) == 0, zb_ = (n & 3) == 1;
      const v4u Az = {za_ ? zz.x : 0u, za_ ? zz.y : 0u, zb_ ? zz.x : 0u, zb_ ? zz.y : 0u};
#pragma unroll
      for (int rt = 0; rt < 2; ++rt) {
        const f32x4 na = mfma(Aa, pq[rt], z4), nb = mfma(Ab, pq[rt], z4), dn = mfma(Az, pq[rt], z4);
        const float ra = __builtin_amdgcn_rcpf(dn[0] + 1e-6f), rb2 = __builtin_amdgcn_rcpf(dn[1] + 1e-6f);
        const v4u o = pair16(pack2(na[0] * ra, na[1] * ra), pack2(na[2] * ra, na[3] * ra), pack2(nb[0] * rb2, nb[1] * rb2), pack2(nb[2] * rb2, nb[3] * rb2));
        const unsigned kg = (q & 1) * 2 + (q >> 1);
        stb1(rs, OFF_O + l * XF_BYTES + (unsigned)((rt0 + rt) * 16 + j) * 1024u + (kg * 16 + n) * 16, o);
      }
      STAMP(SB + 1);
      DRAIN();
      arrive(fb, FC_O + (l * 16 + g) * 4 + w, 0, lane);
      BAR();
      STAMP(SB + 2);
      if constexpr (l == 0) {
        static_for<12>([&](auto K) {
          const unsigned pc = (unsigned)((K & 3) * 4 + w) * 1024u;
          dma16(wsp + OFF_W1 + jo * 114688u + (2 + (K >> 2)) * LU + pc, l16, lbase + W1U[0][2 + (K >> 2)] * LU + pc);
        });
      } else {
        static_for<8>([&](auto K) {
          const unsigned pc = (unsigned)((K & 3) * 4 + w) * 1024u;
          dma16(wsp + OFF_W1 + jo * 114688u + (3 + (K >> 2)) * LU + pc, l16, lbase + W1U[1][3 + (K >> 2)] * LU + pc);
        });
      }
    }
    poll_ge(fb, FC_O + (l * 16 + g) * 4 + ((4 * hh + w) >> 1), 16u, epoch);
    STAMP(SB + 3);
    {
      FRESH();
      v4u a[16];
      const unsigned ab = OFF_O + l * XF_BYTES + (unsigned)(myrt * 16) * 1024u + l16;
      constexpr int AD = 8;
#pragma unroll
      for (int ks = 0; ks < AD; ++ks) a[ks] = ldb1(rs, ab + ks * 1024);
      f32x4 acc[4];
      constexpr int FD = 8;
      v4u wf[FD];
#pragma unroll
      for (int i = 0; i < FD; ++i) wf[i] = ldsf(lb, (WOU[l] + (i & 3)) * LU + (i >> 2) * 1024);
      SCHED();
      static_for<64>([&](auto I) {
        constexpr int i = I;
        const v4u f = wf[i % FD];
        if constexpr (i + FD < 64) wf[i % FD] = ldsf(lb, (WOU[l] + ((i + FD) & 3)) * LU + ((i + FD) >> 2) * 1024);
        mfma1<(i < 4)>(acc[i & 3], f, a[i >> 2]);
        if constexpr ((i & 3) == 1 && (i >> 2) + AD < 16) a[(i >> 2) + AD] = ldb1(rs, ab + ((i >> 2) + AD) * 1024);
        SCHED();
      });
      MFMA_FENCE();
      PINV(acc[0]); PINV(acc[1]); PINV(acc[2]); PINV(acc[3]);
      res_update(res, acc);
      store_x_tile(rs, OFF_X1 + l * XF_BYTES, OFF_ST + l * ST_BYTES, res, myrt, cs, lane);
      STAMP(SB + 4);
      DRAIN();
      arrive(fb, FC_X1 + (l * 16 + g) * 8 + 4 * hh + w, 0, lane);
      BAR();
      STAMP(SB + 5);
      static_for<8>([&](auto K) {
        const unsigned pc = (unsigned)((K & 3) * 4 + w) * 1024u;
        dma16(wsp + OFF_W1 + jo * 114688u + (5 + (K >> 2)) * LU + pc, l16, lbase + W1U[l][5 + (K >> 2)] * LU + pc);
      });
      if constexpr (l == 0) static_for<14>([&](auto I) { ldw2(IC(14 + I), l16); });
    }

    poll_ge(fb, FC_X1 + (l * 16 + g) * 8 + 2 * w, 8u, epoch);
    poll_ge(fb, FC_X1 + (l * 16 + g) * 8 + 2 * w + 1, 8u, epoch);
    STAMP(SB + 6);
    {
      FRESH();
      const int hline_out = FC_H + ((l * 16 + g) * 2 + (w >> 1)) * 2, hline_in = FC_H + ((l * 16 + g) * 2 + hh) * 2;
      const unsigned ab = OFF_X1 + l * XF_BYTES + (unsigned)(rt0 * 16) * 1024u + l16;
      v4u xf[32], sp[2];
      constexpr int AD = 12, FD = 7, NF = 112;
      load_stats(rs, OFF_ST + l * ST_BYTES, rt0, lane, sp);
#pragma unroll
      for (int f = 0; f < AD; ++f) xf[f] = ldb1(rs, ab + f * 1024);
      VMW(AD + 2);
      BAR();
      auto faddr = [&](int i) -> unsigned { return (unsigned)(W1U[l][i % 7] * LU + (i / 7) * 1024); };
      float sa[2], sb[2];
      const unsigned hst = OFF_H + l * HF_BYTES + ((unsigned)rt0 * 56u + (unsigned)(j >> 1) * 7u) * 1024u + ((2 * (j & 1) + (q >> 1)) * 16 + n) * 16;
      v4u ho[2][4];
      auto half1 = [&](auto HH, auto hook) {
        constexpr int H = HH;
        f32x4 acc[7];
        v4u wf[FD];
#pragma unroll
        for (int i = 0; i < FD; ++i) wf[i] = ldsf(lb, faddr(i));
        SCHED();
        static_for<NF>([&](auto I) {
          constexpr int i = I, t = i % 7, ks = i / 7;
          const v4u f = wf[i % FD];
          if constexpr (i + FD < NF) wf[i % FD] = ldsf(lb, faddr(i + FD));
          mfma1<ks == 0>(acc[t], f, xf[H * 16 + ks]);
          hook(I);
          SCHED();
        });
        MFMA_FENCE();
#pragma unroll
        for (int t = 0; t < 7; ++t) PINV(acc[t]);
        if constexpr (H == 0) {
          Stats st;
          make_stats(sp, st);
          sa[0] = st.a[0] * LOG2E; sa[1] = st.a[1] * LOG2E; sb[0] = st.b[0] * LOG2E; sb[1] = st.b[1] * LOG2E;
        }
        unsigned y2[7][2];
#pragma unroll
        for (int t = 0; t < 7; ++t) {
          const f32x4 c = *(LAS f32x4*)(L + L_CS1 + (t * 16 + 4 * q) * 4);
          float y[4];
#pragma unroll
          for (int r = 0; r < 4; ++r) {
            const float v = fmaf(acc[t][r], sa[H], sb[H] * c[r]);
            y[r] = v * __builtin_amdgcn_rcpf(fmaf(__builtin_amdgcn_exp2f(-v), LOG2E, LOG2E));
          }
          y2[t][0] = pack2(y[0], y[1]); y2[t][1] = pack2(y[2], y[3]);
        }
#pragma unroll
        for (int p = 0; p < 3; ++p) ho[H][p] = pair16(y2[2 * p][0], y2[2 * p][1], y2[2 * p + 1][0], y2[2 * p + 1][1]);
        ho[H][3] = v4u{y2[6][0], y2[6][1], 0u, 0u};
      };
      auto hstore = [&](auto HH) {
        constexpr int H = HH;
#pragma unroll
        for (int p = 0; p < 3; ++p) stb1(rs, hst + H * 56 * 1024 + (2 * p + (q & 1)) * 1024, ho[H][p]);
        stb1_64(rs, hst + H * 56 * 1024 + 6 * 1024 + (q & 1) * 8, v2u{ho[H][3].x, ho[H][3].y});
      };
      half1(IC(0), [&](auto I) {
        constexpr int i = I;
        if constexpr (i % 4 == 2 && AD + i / 4 < 32) xf[AD + i / 4] = ldb1(rs, ab + (AD + i / 4) * 1024);
        if constexpr (i == 0) STAMP(SB + 16);
      });
      STAMP(SB + 17);
      hstore(IC(0));
      SCHED();
      half1(IC(1), [&](auto I) {
        constexpr int i = I;
        if constexpr (i == 72) { VMW(0); arrive(fb, hline_out + 0, w & 1, lane); }
        if constexpr (l == 0 && i >= 76 && i < 104) ldw2(IC(28 + i - 76), l16);
      });
      STAMP(SB + 18);
      constexpr int WIN = 16;
      v4u a[WIN];
      f32x4 acc2[2][4];
      auto hoff = [](int f) -> unsigned { return (unsigned)(((f >> 1) & 1) * 7 + (f >> 2)) * 1024u + (unsigned)(f & 1) * (2 * 56 * 1024); };
      auto hbase = [&](int H) -> unsigned { return OFF_H + l * HF_BYTES + (unsigned)((8 * g + 4 * hh + H) * 56 + 14 * w) * 1024u + l16; };
      auto gemm2_start = [&](auto HH) {
        poll_ge2(fb, hline_in + HH, 16u, epoch, lane);
        const unsigned hb = hbase(HH);
#pragma unroll
        for (int f = 0; f < WIN; ++f) a[f] = ldb1(rs, hb + hoff(f));
      };
      auto gemm2 = [&](auto HH, auto hook) {
        const unsigned hb = hbase(HH);
        static_for<28>([&](auto F) {
          constexpr int f = F, wi = 2 * (f >> 2) + ((f >> 1) & 1);
          const v4u cur = a[f % WIN];
          if constexpr (f + WIN < 28) a[f % WIN] = ldb1(rs, hb + hoff(f + WIN));
          mfma4a<(f < 2)>(acc2[f & 1], w2[wi][0], w2[wi][1], w2[wi][2], w2[wi][3], cur);
          hook(F);
          SCHED();
        });
        MFMA_FENCE();
#pragma unroll
        for (int i2 = 0; i2 < 2; ++i2)
#pragma unroll
          for (int c = 0; c < 4; ++c) PINV(acc2[i2][c]);
      };
      auto reduce2 = [&](auto HH) {
        constexpr int H = HH;
#pragma unroll
        for (int i2 = 0; i2 < 2; ++i2) {
          const int own = H + 2 * i2;
          if (w != own) {
            const int slot = w - (w > own ? 1 : 0);
#pragma unroll
            for (int c = 0; c < 4; ++c) *(LAS f32x4*)(L + REDU[l][i2] * LU + (slot * 4 + c) * 1024 + l16) = acc2[i2][c];
          }
        }
        BAR();
        if ((w & 1) == H) {
          f32x4 s[4];
          const unsigned ru = (w >> 1) ? (unsigned)REDU[l][1] * LU : (unsigned)REDU[l][0] * LU;
#pragma unroll
          for (int c = 0; c < 4; ++c) s[c] = (w >> 1) ? acc2[1][c] : acc2[0][c];
#pragma unroll
          for (int sl = 0; sl < 3; ++sl)
#pragma unroll
            for (int c = 0; c < 4; ++c) s[c] += *(LAS f32x4*)(L + ru + (sl * 4 + c) * 1024 + l16);
          res_update(res, s);
          if constexpr (l == 0) {
            store_x_tile(rs, OFF_X2, OFF_ST + 2 * ST_BYTES, res, myrt, cs, lane);
          } else {
#pragma unroll
            for (int c2 = 0; c2 < 2; ++c2) {
              const v4u o = pair16(res[2 * c2].x, res[2 * c2].y, res[2 * c2 + 1].x, res[2 * c2 + 1].y);
              *(GAS v4u*)(out + (size_t)(16 * myrt + n) * 512 + 64 * cs + 16 * (2 * c2 + (q & 1)) + 8 * (q >> 1)) = o;
            }
          }
        }
      };
      gemm2_start(IC(0));
      hstore(IC(1));
      STAMP(SB + 7);
      BAR();
      gemm2(IC(0), [&](auto F) {
        constexpr int f = F;
        if constexpr (f == 14) { VMW(12 + (l == 0 ? 5 : 0)); arrive(fb, hline_out + 1, w & 1, lane); }
        if constexpr (l == 0 && f >= 4 && (f & 1) == 0) {
          const unsigned o = (unsigned)(((f - 4) >> 1) * 4 + w) * 1024u;
          dma16(wsp + OFF_WQKV + jo * 98304u + o, l16, lbase + QU * LU + o);
        }
      });
      STAMP(SB + 9);
      gemm2_start(IC(1));
      reduce2(IC(0));
      STAMP(SB + 10);
      gemm2(IC(1), [&](auto F) {
        constexpr int f = F;
        if constexpr (l == 0 && f == 14) { VMW(17); if ((w & 1) == 0) arrive(fb, FC_X2 + g * 8 + 4 * hh + w, 0, lane); }
        if constexpr (l == 0 && f >= 4 && (f & 1) == 0) {
          const unsigned o = (unsigned)(48 + ((f - 4) >> 1) * 4 + w) * 1024u;
          dma16(wsp + OFF_WQKV + jo * 98304u + o, l16, lbase + QU * LU + o);
        }
      });
      BAR();
      reduce2(IC(1));
      STAMP(SB + 11);
      if constexpr (l == 0) {
        DRAIN();
        if ((w & 1) == 1) arrive(fb, FC_X2 + g * 8 + 4 * hh + w, 0, lane);
      }
    }
    if constexpr (l == 0) {
      poll_ge(fb, FC_X2 + g * 8 + 2 * w, 8u, epoch);
      BAR();
      STAMP(SB + 13);
      FRESH();
      const unsigned ab = OFF_X2 + (unsigned)(rt0 * 16) * 1024u + l16;
      v4u xf[32], sp[2];
      constexpr int AD = 12, FD = 6, NF = 96;
      sp[0] = ldb1(rs, OFF_ST + 2 * ST_BYTES + (unsigned)rt0 * 1024u + n * 64 + q * 16);
#pragma unroll
      for (int f = 0; f < AD; ++f) xf[f] = ldb1(rs, ab + f * 1024);
      f32x4 acc[6][2];
      Stats st;
      auto halfq = [&](auto HH, auto hook) {
        constexpr int H = HH;
        auto faddr = [&](int i) -> unsigned { return (unsigned)((QU + i % 6) * LU + (i / 6) * 1024); };
        v4u wf[FD];
#pragma unroll
        for (int i = 0; i < FD; ++i) wf[i] = ldsf(lb, faddr(i));
        SCHED();
        static_for<NF>([&](auto I) {
          constexpr int i = I, t = i % 6, ks = i / 6;
          const v4u f = wf[i % FD];
          if constexpr (i + FD < NF) wf[i % FD] = ldsf(lb, faddr(i + FD));
          if constexpr (t < 4) mfma1<ks == 0>(acc[t][H], xf[H * 16 + ks], f);
          else mfma1<ks == 0>(acc[t][H], f, xf[H * 16 + ks]);
          hook(I);
          SCHED();
        });
      };
      halfq(IC(0), [&](auto I) {
        constexpr int i = I;
        if constexpr (i % 12 == 2 && AD + i / 12 * 1 < 16) xf[AD + i / 12] = ldb1(rs, ab + (AD + i / 12) * 1024);
        if constexpr (i == 48) {
          poll_ge(fb, FC_X2 + g * 8 + 2 * w + 1, 8u, epoch);
          sp[1] = ldb1(rs, OFF_ST + 2 * ST_BYTES + (unsigned)(rt0 + 1) * 1024u + n * 64 + q * 16);
        }
        if constexpr (i > 48 && i <= 80 && (i & 1) == 0) xf[16 + (i - 50) / 2] = ldb1(rs, ab + (16 + (i - 50) / 2) * 1024);
      });
      halfq(IC(1), [&](auto I) {
        constexpr int i = I;
        if constexpr (i == 40) {
          make_stats(sp, st);
          if (q == 0) {
            *(LAS f32x2*)(L + L_STX + w * 256 + n * 8) = f32x2{st.a[0], st.b[0]};
            *(LAS f32x2*)(L + L_STX + w * 256 + 128 + n * 8) = f32x2{st.a[1], st.b[1]};
          }
        }
        if constexpr ((i & 7) == 4) {
          constexpr int k = i >> 3;
          const unsigned pc = (unsigned)((k & 3) * 4 + w) * 1024u;
          dma16(wsp + OFF_W1 + jo * 114688u + (k >> 2) * LU + pc, l16, lbase + W1U[1][k >> 2] * LU + pc);
        }
      });
      MFMA_FENCE();
#pragma unroll
      for (int t = 0; t < 6; ++t) { PINV(acc[t][0]); PINV(acc[t][1]); }
      f32x4 kva, kvb, za, zb;
      kv_epilogue(acc, L + L_STX + w * 256, (LAS float*)(L + L_CSQ), lane, kva, kvb, za, zb);
      kv_publish(L, L_KVX, rs, OFF_KVP + (unsigned)((16 + j) * 16 + g) * KVREC, lane, w, kva, kvb, za, zb);
      STAMP(SB + 14);
      publish(fb, FC_KV + (16 + j) * 2, tid);
      STAMP(SB + 15);
      dma_w123<64>(ws, OFF_WO + cs * 65536u, lbase + WOU[1] * LU, w, lane0);
      q_epilogue(acc, st, (LAS float*)(L + L_CSQ), lane, pq);
      if (b == 0 && tid == 0) __hip_atomic_store(fb + FO_EPOCH / 4, epoch, RLX_AGENT);
    }
  });
  STAMP_FLUSH(0, 64);
}

}

extern "C" void kernel_launch(void* const* d_in, const int* in_sizes, int n_in, void* d_out, int out_size, void* d_ws, size_t ws_size,
                              hipStream_t stream) {
  const u16* x = (const u16*)d_in[0];
  const u16* wqkv = (const u16*)d_in[1];
  const u16* wo = (const u16*)d_in[2];
  const u16* w1 = (const u16*)d_in[4];
  const u16* w2 = (const u16*)d_in[5];
  (void)hipFuncSetAttribute((const void*)kmain, hipFuncAttributeMaxDynamicSharedMemorySize, K2_LDS);
  kmain<<<256, 256, K2_LDS, stream>>>(x, wqkv, wo, w1, w2, (u16*)d_out, (unsigned char*)d_ws);
}
```

```cpp
#include <hip/hip_runtime.h>
#include <stdint.h>
#include <utility>

namespace {

typedef unsigned v4u __attribute__((ext_vector_type(4)));
typedef unsigned v2u __attribute__((ext_vector_type(2)));
typedef float f32x4 __attribute__((ext_vector_type(4)));
typedef float f32x2 __attribute__((ext_vector_type(2)));
typedef __bf16 bf2 __attribute__((ext_vector_type(2)));
typedef __bf16 bf8 __attribute__((ext_vector_type(8)));
typedef short s4v __attribute__((ext_vector_type(4)));
typedef unsigned short u16;
#define DI __device__ __forceinline__
#define GAS __attribute__((address_space(1)))
#define LAS __attribute__((address_space(3)))
typedef GAS unsigned gu32;
#define RLX_AGENT __ATOMIC_RELAXED, __HIP_MEMORY_SCOPE_AGENT
typedef __amdgpu_buffer_rsrc_t rsrc_t;

constexpr unsigned OFF_FLAGS = 0;
constexpr unsigned OFF_WQKV = 124928;
constexpr unsigned OFF_WO = OFF_WQKV + 1536u * 1024u;
constexpr unsigned OFF_W1 = OFF_WO + 512u * 1024u;
constexpr unsigned OFF_W2 = OFF_W1 + 1792u * 1024u;
constexpr unsigned OFF_CS1 = OFF_W2 + 1792u * 1024u;
constexpr unsigned OFF_CSQ = OFF_CS1 + 8u * 1792u * 4u;
constexpr unsigned OFF_PQ = OFF_CSQ + 16u * 96u * 4u;
constexpr unsigned KVREC = 2304;
constexpr unsigned OFF_KVP = OFF_PQ + 256u * 8192u;
constexpr unsigned OFF_ST = OFF_KVP + 2u * 256u * KVREC;
constexpr unsigned ST_BYTES = 128u * 1024u;
constexpr unsigned OFF_O = OFF_ST + 3u * ST_BYTES;
constexpr unsigned XF_BYTES = 128u * 16u * 1024u;
constexpr unsigned OFF_X1 = OFF_O + 2u * XF_BYTES;
constexpr unsigned OFF_X2 = OFF_X1 + 2u * XF_BYTES;
constexpr unsigned HF_BYTES = 128u * 56u * 1024u;
constexpr unsigned OFF_H = OFF_X2 + XF_BYTES;
constexpr unsigned WS_BYTES = OFF_H + 2u * HF_BYTES;
static_assert(OFF_O % 256 == 0 && OFF_KVP % 256 == 0 && OFF_PQ % 256 == 0 && OFF_ST % 256 == 0, "align");

constexpr unsigned FO_PRE = 0, FO_EPOCH = 1024, FO_TMO = 1088, FO_CNT = 2048;
constexpr int FC_O = 0;
constexpr int FC_X1 = 128;
constexpr int FC_H = 704;
constexpr int FC_X2 = 512;
constexpr int FC_KV = 640;
constexpr unsigned FLAG_BYTES = 2048 + 256 * 480;
static_assert(FO_CNT + (FC_H + 256) * 128 <= FLAG_BYTES, "flags");
constexpr unsigned SPIN_CAP = 1u << 17;

constexpr int AUX_SC1 = 16;

constexpr unsigned LU = 16384;
constexpr unsigned L_SCR = 9 * LU;
constexpr unsigned L_CS1 = L_SCR;
constexpr unsigned L_CSQ = L_SCR + 512;
constexpr unsigned L_KVX = L_SCR + 1024;
constexpr unsigned L_CSP = L_SCR + 9728;
constexpr unsigned L_STX = L_SCR + 11264;
constexpr unsigned L_STAMP = L_SCR + 12288;
constexpr unsigned L_XB = 6 * LU;
constexpr int W1U[2][7] = {{4, 5, 6, 7, 8, 0, 1}, {0, 1, 8, 6, 7, 2, 3}};
constexpr int WOU[2] = {0, 2};
constexpr int QU = 2;
constexpr int REDU[2][3] = {{0, 1, 8}, {4, 5, 6}};
constexpr unsigned K2_LDS = 10 * LU;

DI f32x4 mfma(v4u a, v4u b, f32x4 c) {
  return __builtin_amdgcn_mfma_f32_16x16x32_bf16(__builtin_bit_cast(bf8, a), __builtin_bit_cast(bf8, b), c, 0, 0, 0);
}
template <bool FIRST> DI void mfma2(f32x4& c0, f32x4& c1, v4u a0, v4u b0, v4u a1, v4u b1) {
  if constexpr (FIRST)
    asm volatile("v_mfma_f32_16x16x32_bf16 %0, %2, %3, 0\n\tv_mfma_f32_16x16x32_bf16 %1, %4, %5, 0"
                 : "=&v"(c0), "=&v"(c1) : "v"(a0), "v"(b0), "v"(a1), "v"(b1));
  else
    asm volatile("v_mfma_f32_16x16x32_bf16 %0, %2, %3, %0\n\tv_mfma_f32_16x16x32_bf16 %1, %4, %5, %1"
                 : "+v"(c0), "+v"(c1) : "v"(a0), "v"(b0), "v"(a1), "v"(b1));
}
template <bool FIRST> DI void mfma1(f32x4& c0, v4u a0, v4u b0) {
  if constexpr (FIRST) asm volatile("v_mfma_f32_16x16x32_bf16 %0, %1, %2, 0" : "=&v"(c0) : "v"(a0), "v"(b0));
  else asm volatile("v_mfma_f32_16x16x32_bf16 %0, %1, %2, %0" : "+v"(c0) : "v"(a0), "v"(b0));
}
template <bool FIRST> DI void mfma4a(f32x4 (&c)[4], v4u w0, v4u w1, v4u w2, v4u w3, v4u b) {
  if constexpr (FIRST)
    asm volatile("s_nop 1\n\tv_mfma_f32_16x16x32_bf16 %0, %4, %8, 0\n\tv_mfma_f32_16x16x32_bf16 %1, %5, %8, 0\n\t"
                 "v_mfma_f32_16x16x32_bf16 %2, %6, %8, 0\n\tv_mfma_f32_16x16x32_bf16 %3, %7, %8, 0"
                 : "=&v"(c[0]), "=&v"(c[1]), "=&v"(c[2]), "=&v"(c[3]) : "a"(w0), "a"(w1), "a"(w2), "a"(w3), "v"(b));
  else
    asm volatile("s_nop 1\n\tv_mfma_f32_16x16x32_bf16 %0, %4, %8, %0\n\tv_mfma_f32_16x16x32_bf16 %1, %5, %8, %1\n\t"
                 "v_mfma_f32_16x16x32_bf16 %2, %6, %8, %2\n\tv_mfma_f32_16x16x32_bf16 %3, %7, %8, %3"
                 : "+v"(c[0]), "+v"(c[1]), "+v"(c[2]), "+v"(c[3]) : "a"(w0), "a"(w1), "a"(w2), "a"(w3), "v"(b));
}
#define MFMA_FENCE() asm volatile("s_nop 15\n\ts_nop 3" ::: "memory")
DI void pinv(f32x4& x) { asm volatile("" : "+v"(x)); }
#define PINV(x) pinv(x)
DI unsigned pack2(float a, float b) {
  f32x2 v = {a, b};
  return __builtin_bit_cast(unsigned, __builtin_convertvector(v, bf2));
}
DI float bflo(unsigned u) { return __builtin_bit_cast(float, u << 16); }
DI float bfhi(unsigned u) { return __builtin_bit_cast(float, u & 0xffff0000u); }
DI float dot2(unsigned a, unsigned b, float c) {
  return __builtin_amdgcn_fdot2_f32_bf16(__builtin_bit_cast(bf2, a), __builtin_bit_cast(bf2, b), c, false);
}
constexpr float LOG2E = 1.4426950408889634f;
DI float phi(float t) { return __builtin_amdgcn_exp2f(fminf(t, 0.f) * LOG2E) + (fmaxf(t, 0.f) + 1e-6f); }
DI float silu(float t) { return t * __builtin_amdgcn_rcpf(1.0f + __builtin_amdgcn_exp2f(t * -LOG2E)); }
DI v4u ldb(rsrc_t rs, unsigned off) { return __builtin_amdgcn_raw_buffer_load_b128(rs, off, 0, 0); }
DI v4u ldb1(rsrc_t rs, unsigned off) { return __builtin_amdgcn_raw_buffer_load_b128(rs, off, 0, AUX_SC1); }
DI v4u lda128(v4u srd, unsigned off) {
  v4u t;
  asm volatile("buffer_load_dwordx4 %0, %1, %2, 0 offen sc1" : "=a"(t) : "v"(off), "s"(srd) : "memory");
  return t;
}
DI void stb1(rsrc_t rs, unsigned off, v4u v) { __builtin_amdgcn_raw_buffer_store_b128(v, rs, off, 0, AUX_SC1); }
DI void stb1_64(rsrc_t rs, unsigned off, v2u v) { __builtin_amdgcn_raw_buffer_store_b64(v, rs, off, 0, AUX_SC1); }
#define DRAIN() asm volatile("s_waitcnt vmcnt(0)" ::: "memory")
#define LGKM0() asm volatile("s_waitcnt lgkmcnt(0)" ::: "memory")
#define BAR() do { asm volatile("s_waitcnt lgkmcnt(0)" ::: "memory"); __builtin_amdgcn_s_barrier(); asm volatile("" ::: "memory"); } while (0)
#define SCHED() __builtin_amdgcn_sched_barrier(0)
#define PIN(x) asm volatile("" : "+v"(x))
#define PIN4(a, o) asm volatile("" : "+v"(a[(o)]), "+v"(a[(o) + 1]), "+v"(a[(o) + 2]), "+v"(a[(o) + 3]))
#define STAMP(i) do {} while (0)
#define STAMP_INIT() do {} while (0)
#define STAMP_FLUSH(lo, hi) do {} while (0)

template <int... I, typename F> DI void static_for_impl(std::integer_sequence<int, I...>, F&& f) { (f(std::integral_constant<int, I>{}), ...); }
template <int N, typename F> DI void static_for(F&& f) { static_for_impl(std::make_integer_sequence<int, N>{}, f); }

DI void dma16(const void* sbase, unsigned voff, unsigned lds_dst) {
  unsigned keep;
  asm volatile("s_mov_b32 %0, m0\n\ts_mov_b32 m0, %3\n\ts_nop 3\n\tglobal_load_lds_dwordx4 %1, %2 sc1\n\ts_mov_b32 m0, %0"
               : "=&s"(keep) : "v"(voff), "s"(sbase), "s"(lds_dst) : "memory");
}
DI void dma16_in(const void* sbase, unsigned voff, unsigned lds_dst) {
  unsigned keep;
  asm volatile("s_mov_b32 %0, m0\n\ts_mov_b32 m0, %3\n\ts_nop 3\n\tglobal_load_lds_dwordx4 %1, %2\n\ts_mov_b32 m0, %0"
               : "=&s"(keep) : "v"(voff), "s"(sbase), "s"(lds_dst) : "memory");
}
DI v4u tr_frag(LAS unsigned char* p) {
  s4v lo = __builtin_amdgcn_ds_read_tr16_b64_v4i16((LAS s4v*)(p));
  s4v hi = __builtin_amdgcn_ds_read_tr16_b64_v4i16((LAS s4v*)(p + 256));
  v2u l2 = __builtin_bit_cast(v2u, lo), h2 = __builtin_bit_cast(v2u, hi);
  return v4u{l2.x, l2.y, h2.x, h2.y};
}
DI float sum16(float v) {
  const unsigned u = __builtin_bit_cast(unsigned, v);
  unsigned u2 = u; PIN(u2);
  auto r = __builtin_amdgcn_permlane16_swap(u, u2, false, false);
  const unsigned a = r[0], b = r[1];
  return __builtin_bit_cast(float, a) + __builtin_bit_cast(float, b);
}
DI float sum32(float v) {
  const unsigned u = __builtin_bit_cast(unsigned, v);
  unsigned u2 = u; PIN(u2);
  auto r = __builtin_amdgcn_permlane32_swap(u, u2, false, false);
  const unsigned a = r[0], b = r[1];
  return __builtin_bit_cast(float, a) + __builtin_bit_cast(float, b);
}
DI float sum_q(float v) { return sum32(sum16(v)); }
DI v4u pair16(unsigned a0, unsigned a1, unsigned b0, unsigned b1) {
  auto r0 = __builtin_amdgcn_permlane16_swap(a0, b0, false, false);
  auto r1 = __builtin_amdgcn_permlane16_swap(a1, b1, false, false);
  const unsigned x0 = r0[0], x1 = r1[0], x2 = r0[1], x3 = r1[1];
  return v4u{x0, x1, x2, x3};
}

DI bool spin_fail(unsigned& sp, gu32* fb, unsigned epoch) {
  if (++sp > SPIN_CAP) { __hip_atomic_store(fb + FO_TMO / 4, epoch, RLX_AGENT); return true; }
  return (sp & 255u) == 0u && __hip_atomic_load(fb + FO_TMO / 4, RLX_AGENT) == epoch;
}
DI void poll_ge(gu32* fb, int line, unsigned want, unsigned epoch) {
  gu32* wd = fb + (FO_CNT / 4) + line * 32;
  for (unsigned sp = 0;;) {
    if ((unsigned)__builtin_amdgcn_readfirstlane(__hip_atomic_load(wd, RLX_AGENT)) >= want) return;
    __builtin_amdgcn_s_sleep(1);
    if (spin_fail(sp, fb, epoch)) return;
  }
}
DI void poll_ge2(gu32* fb, int line, unsigned want, unsigned epoch, int lane) {
  gu32* wd = fb + (FO_CNT / 4) + line * 32 + (lane & 1);
  for (unsigned sp = 0;;) {
    if (__all(__hip_atomic_load(wd, RLX_AGENT) >= want)) return;
    __builtin_amdgcn_s_sleep(1);
    if (spin_fail(sp, fb, epoch)) return;
  }
}
DI void poll_dw(gu32* fb, int line, int dw, unsigned want, unsigned epoch) {
  gu32* wd = fb + (FO_CNT / 4) + line * 32 + dw;
  for (unsigned sp = 0;;) {
    if ((unsigned)__builtin_amdgcn_readfirstlane(__hip_atomic_load(wd, RLX_AGENT)) >= want) return;
    __builtin_amdgcn_s_sleep(1);
    if (spin_fail(sp, fb, epoch)) return;
  }
}
DI void arrive(gu32* fb, int line, int dw, int lane) { if (lane == 0) __hip_atomic_fetch_add(fb + (FO_CNT / 4) + line * 32 + dw, 1u, RLX_AGENT); }

struct Stats { float a[2], b[2]; };
DI void finish_stats(float s1, float s2, float& a, float& b) {
  const float mean = s1 * (1.0f / 512.0f);
  const float var = fmaxf(s2 * (1.0f / 512.0f) - mean * mean, 0.f);
  a = __builtin_amdgcn_rsqf(var + 1e-5f);
  b = -mean * a;
}

DI void kv_epilogue(const f32x4 (&acc)[6][2], LAS unsigned char* stx, LAS float* csq, int lane, f32x4& kva, f32x4& kvb, f32x4& za, f32x4& zb) {
  const int q = lane >> 4, n = lane & 15;
  f32x4 ab[2][2];
#pragma unroll
  for (int rt = 0; rt < 2; ++rt) { ab[rt][0] = *(LAS f32x4*)(stx + rt * 128 + q * 32); ab[rt][1] = *(LAS f32x4*)(stx + rt * 128 + q * 32 + 16); }
  v4u PK[2], V[2];
#pragma unroll
  for (int h = 0; h < 2; ++h) {
    const float ck = csq[(2 * h) * 16 + n], cv = csq[(2 * h + 1) * 16 + n];
    unsigned pk[4], pv[4];
#pragma unroll
    for (int rt = 0; rt < 2; ++rt) {
      float a[4], b[4];
#pragma unroll
      for (int r = 0; r < 4; ++r) {
        const float sa = ab[rt][r >> 1][(r & 1) * 2], sb = ab[rt][r >> 1][(r & 1) * 2 + 1];
        a[r] = phi(fmaf(acc[2 * h][rt][r], sa, sb * ck));
        b[r] = fmaf(acc[2 * h + 1][rt][r], sa, sb * cv);
      }
      pk[2 * rt] = pack2(a[0], a[1]); pk[2 * rt + 1] = pack2(a[2], a[3]);
      pv[2 * rt] = pack2(b[0], b[1]); pv[2 * rt + 1] = pack2(b[2], b[3]);
    }
    PK[h] = v4u{pk[0], pk[1], pk[2], pk[3]};
    V[h] = v4u{pv[0], pv[1], pv[2], pv[3]};
  }
  const f32x4 z4 = {0, 0, 0, 0};
  const v4u ones = {0x3f803f80u, 0x3f803f80u, 0x3f803f80u, 0x3f803f80u};
  kva = mfma(PK[0], V[0], z4); kvb = mfma(PK[1], V[1], z4);
  za = mfma(PK[0], ones, z4); zb = mfma(PK[1], ones, z4);
}
DI void q_epilogue(const f32x4 (&acc)[6][2], const Stats& st, LAS float* csq, int lane, v4u (&pq)[2]) {
  const int q = lane >> 4;
#pragma unroll
  for (int rt = 0; rt < 2; ++rt) {
    unsigned p[4];
#pragma unroll
    for (int t = 0; t < 2; ++t) {
      const f32x4 c = *(LAS f32x4*)(csq + (4 + t) * 16 + 4 * q);
      float v[4];
#pragma unroll
      for (int r = 0; r < 4; ++r) v[r] = phi(fmaf(acc[4 + t][rt][r], st.a[rt], st.b[rt] * c[r]));
      p[2 * t] = pack2(v[0], v[1]); p[2 * t + 1] = pack2(v[2], v[3]);
    }
    pq[rt] = v4u{p[0], p[1], p[2], p[3]};
  }
}

DI void kv_publish(LAS unsigned char* L, unsigned kvx, rsrc_t rs, unsigned rec_off, int lane, int w, f32x4 kva, f32x4 kvb, f32x4 za, f32x4 zb) {
  LAS unsigned char* me = L + kvx + w * 2176;
  *(LAS f32x4*)(me + lane * 16) = kva;
  *(LAS f32x4*)(me + 1024 + lane * 16) = kvb;
  if ((lane & 15) == 0) {
    *(LAS f32x4*)(me + 2048 + (lane >> 4) * 16) = za;
    *(LAS f32x4*)(me + 2112 + (lane >> 4) * 16) = zb;
  }
  BAR();
  if (w < 2 || (w == 2 && lane < 8)) {
    const unsigned o = w * 1024 + lane * 16;
    f32x4 s = *(LAS f32x4*)(L + kvx + o);
#pragma unroll
    for (int ww = 1; ww < 4; ++ww) s += *(LAS f32x4*)(L + kvx + ww * 2176 + o);
    stb1(rs, rec_off + o, __builtin_bit_cast(v4u, s));
  }
}

struct LBase { LAS unsigned char* b[3]; };
DI v4u ldsf(const LBase& lb, unsigned off) { return *(LAS v4u*)(lb.b[off >> 16] + (off & 0xffffu)); }

DI void publish(gu32* fb, int line, int tid) {
  DRAIN();
  BAR();
  arrive(fb, line, 0, tid);
}
template <int NKB>
DI void dma_block(const unsigned char* ws, unsigned src, unsigned ldst, int w, int lane) {
  static_assert(NKB % 16 == 0, "");
  const unsigned char* p = ws + src + (unsigned)w * (NKB / 4) * 1024u;
  unsigned d = ldst + (unsigned)w * (NKB / 4) * 1024u;
#pragma unroll 1
  for (int i = 0; i < NKB / 16; ++i) {
    dma16(p, (unsigned)lane * 16u, d);
    dma16(p + 1024, (unsigned)lane * 16u, d + 1024);
    dma16(p + 2048, (unsigned)lane * 16u, d + 2048);
    dma16(p + 3072, (unsigned)lane * 16u, d + 3072);
    p += 4096; d += 4096;
  }
}
template <int NKB>
DI void dma_w123(const unsigned char* ws, unsigned src, unsigned ldst, int w, int lane) {
  if (w == 0) return;
  const unsigned char* p = ws + src + (unsigned)(w - 1) * 1024u;
  unsigned d = ldst + (unsigned)(w - 1) * 1024u;
#pragma unroll 1
  for (int i = w - 1; i < NKB; i += 3) {
    dma16(p, (unsigned)lane * 16u, d);
    p += 3072; d += 3072;
  }
}
template <int NT, int NTN, int AD, int FD, typename FA, typename HOOK>
DI void gemm_k(rsrc_t rs, unsigned ab, const LBase& lb, f32x4 (&acc)[NT][2], FA faddr, HOOK hook) {
  constexpr int R = AD + 1, NF = 16 * NT;
  v4u xa[R], xb[R];
#pragma unroll
  for (int ks = 0; ks < AD; ++ks) { xa[ks] = ldb1(rs, ab + ks * 1024); xb[ks] = ldb1(rs, ab + (16 + ks) * 1024); }
  v4u wf[FD];
#pragma unroll
  for (int i = 0; i < FD; ++i) wf[i] = ldsf(lb, faddr(i % NT, i / NT));
  SCHED();
  static_for<NF>([&](auto I) {
    constexpr int i = I, t = i % NT, ks = i / NT;
    const v4u f = wf[i % FD];
    if constexpr (i + FD < NF) wf[i % FD] = ldsf(lb, faddr((i + FD) % NT, (i + FD) / NT));
    if constexpr (t < NTN) mfma2<ks == 0>(acc[t][0], acc[t][1], xa[ks % R], f, xb[ks % R], f);
    else mfma2<ks == 0>(acc[t][0], acc[t][1], f, xa[ks % R], f, xb[ks % R]);
    if constexpr (t == 1 && ks + AD < 16) { xa[(ks + AD) % R] = ldb1(rs, ab + (ks + AD) * 1024); xb[(ks + AD) % R] = ldb1(rs, ab + (16 + ks + AD) * 1024); }
    hook(I);
    SCHED();
  });
  MFMA_FENCE();
#pragma unroll
  for (int t = 0; t < NT; ++t) { PINV(acc[t][0]); PINV(acc[t][1]); }
}
DI void res_update(v2u (&res)[4], const f32x4 (&acc)[4]) {
#pragma unroll
  for (int c = 0; c < 4; ++c) {
    const unsigned t0 = pack2(acc[c][0], acc[c][1]), t1 = pack2(acc[c][2], acc[c][3]);
    res[c].x = pack2(bflo(res[c].x) + bflo(t0), bfhi(res[c].x) + bfhi(t0));
    res[c].y = pack2(bflo(res[c].y) + bflo(t1), bfhi(res[c].y) + bfhi(t1));
  }
}
DI void store_x_tile(rsrc_t rs, unsigned xoff, unsigned stoff, const v2u (&res)[4], int myrt, int cs, int lane) {
  const int q = lane >> 4, n = lane & 15;
  const unsigned kg = (q & 1) * 2 + (q >> 1);
#pragma unroll
  for (int c2 = 0; c2 < 2; ++c2) {
    const v4u o = pair16(res[2 * c2].x, res[2 * c2].y, res[2 * c2 + 1].x, res[2 * c2 + 1].y);
    stb1(rs, xoff + (unsigned)(myrt * 16 + 2 * cs + c2) * 1024u + (kg * 16 + n) * 16, o);
  }
  float s1 = 0, s2 = 0;
#pragma unroll
  for (int c = 0; c < 4; ++c) {
    s1 = dot2(res[c].x, 0x3f803f80u, s1); s2 = dot2(res[c].x, res[c].x, s2);
    s1 = dot2(res[c].y, 0x3f803f80u, s1); s2 = dot2(res[c].y, res[c].y, s2);
  }
  s1 = sum_q(s1); s2 = sum_q(s2);
  if (q == 0) stb1_64(rs, stoff + (unsigned)myrt * 1024u + n * 64 + cs * 8, __builtin_bit_cast(v2u, f32x2{s1, s2}));
}
DI void load_stats(rsrc_t rs, unsigned stoff, int rt0, int lane, v4u (&sp)[2]) {
  const int q = lane >> 4, n = lane & 15;
#pragma unroll
  for (int rt = 0; rt < 2; ++rt) sp[rt] = ldb1(rs, stoff + (unsigned)(rt0 + rt) * 1024u + n * 64 + q * 16);
}
DI void make_stats(const v4u (&sp)[2], Stats& st) {
#pragma unroll
  for (int rt = 0; rt < 2; ++rt) {
    const f32x4 p = __builtin_bit_cast(f32x4, sp[rt]);
    finish_stats(sum_q(p[0] + p[2]), sum_q(p[1] + p[3]), st.a[rt], st.b[rt]);
  }
}

__global__ void __launch_bounds__(256, 1) kmain(const u16* __restrict__ x, const u16* __restrict__ wqkv, const u16* __restrict__ wo,
                                                const u16* __restrict__ w1, const u16* __restrict__ w2g, u16* __restrict__ out,
                                                unsigned char* __restrict__ ws) {
  extern __shared__ __attribute__((aligned(16))) unsigned char smem[];
  LAS unsigned char* L = (LAS unsigned char*)smem;
  constexpr unsigned LSTAMP = L_STAMP;
  const unsigned lbase = (unsigned)(size_t)L;
  const int tid = threadIdx.x, lane0 = tid & 63, w = __builtin_amdgcn_readfirstlane(tid >> 6);
  const int b = blockIdx.x;
  const rsrc_t rs = __builtin_amdgcn_make_buffer_rsrc((void*)ws, 0, WS_BYTES, 0x00020000);
  const v4u srd = {(unsigned)(size_t)ws, (unsigned)((size_t)ws >> 32) & 0xffffu, WS_BYTES, 0x00020000u};
  gu32* fb = (gu32*)(ws + OFF_FLAGS);
  const f32x4 z4 = {0, 0, 0, 0};
  const int gm = 4 * ((b & 7) >> 1) + (b >> 6), jm = 8 * (b & 1) + ((b >> 3) & 7);
  const int hh = (jm >> 2) & 1, cs = (jm & 3) | ((jm >> 3) << 2);
#define FRESH() int lane = lane0; PIN(lane); const int q = lane >> 4, n = lane & 15; const unsigned l16 = (unsigned)lane * 16u; \
  LBase lb; { unsigned o1 = l16 + 65536u, o2 = l16 + 131072u; PIN(o1); PIN(o2); lb.b[0] = L + l16; lb.b[1] = L + o1; lb.b[2] = L + o2; } (void)q; (void)n; (void)lb; \
  const unsigned char* wsp = ws; asm volatile("" : "+s"(wsp)); int jo = jm; asm volatile("" : "+s"(jo)); (void)wsp; (void)jo;
  STAMP_INIT();
  STAMP(0);
  const unsigned epoch = __hip_atomic_load(fb + FO_EPOCH / 4, RLX_AGENT) + 1u;

  v4u w2[14][4];
  auto ldw2 = [&](auto I, unsigned l16) {
    constexpr int i = I;
    w2[i >> 2][i & 3] = ldb1(rs, OFF_W2 + (unsigned)((cs * 4 + w) * 56 + i) * 1024u + l16);
  };
  {
    FRESH();
    const int g = 4 * ((b & 7) >> 1) + (b >> 6), j = 8 * (b & 1) + ((b >> 3) & 7);
    const int drow = lane >> 2, dch = (lane & 3) ^ (((drow >> 3) & 1) << 1);
    {
      const unsigned voff = (unsigned)(drow * 1536 + 32 * j + dch * 8) * 2u;
#pragma unroll
      for (int i = 0; i < 24; ++i) {
        const int it = 4 * i + w, rb = it / 3, seg = it % 3;
        dma16_in((const unsigned char*)wqkv + (size_t)(rb * 16 * 1536 + seg * 512) * 2, voff, lbase + (seg * 32 + rb) * 1024);
      }
    }
    v4u xr[4][8];
    {
      const GAS v4u* xp = (const GAS v4u*)(x + (size_t)(128 * g + 32 * w + (lane >> 5)) * 512 + (lane & 31) * 8);
#pragma unroll
      for (int blk = 0; blk < 4; ++blk)
#pragma unroll
        for (int i = 0; i < 8; ++i) xr[blk][i] = xp[((blk >> 1) * 16 + 2 * i) * 64 + (blk & 1) * 32];
    }
    const u16* psrc; int pld, pp, pks, pkind, prow[4];
    if (b < 112) { psrc = w1; pld = 1792; pp = b >> 1; pks = 8 * (b & 1) + 2 * w; pkind = 0; }
    else {
      const int u = (b - 112) * 8 + 2 * w;
      if (u < 896) { psrc = w2g; pld = 512; pp = u / 56; pks = u % 56; pkind = 1; }
      else { psrc = wo; pld = 512; pp = (u - 896) >> 4; pks = (u - 896) & 15; pkind = 2; }
    }
#pragma unroll
    for (int i = 0; i < 4; ++i) {
      const int ks = pks + (i >> 1);
      prow[i] = pkind == 1 ? 112 * (2 * (ks / 7) + (i & 1)) + 16 * (ks % 7) : 32 * ks + 16 * (i & 1);
    }
    v4u pt[4];
#pragma unroll
    for (int i = 0; i < 4; ++i) pt[i] = *(const GAS v4u*)(psrc + (size_t)(prow[i] + drow) * pld + 32 * pp + dch * 8);
    SCHED();
    if (tid < 30) stb1(rs, OFF_FLAGS + FO_CNT + b * 480 + tid * 16, v4u{0u, 0u, 0u, 0u});
    STAMP(1);

    v4u xa[16], xb[16];
    float s1a = 0, s2a = 0, s1b = 0, s2b = 0;
    LAS unsigned char* B = L + L_XB + w * 8192;
    {
      const int c = lane & 31, ksl = c >> 2, kgw = c & 3, fw = (ksl & 1) * 4 + kgw;
      LAS unsigned char* wp = B + ksl * 1024 + kgw * 256;
      LAS unsigned char* rp0 = B + q * 256 + ((n ^ q) * 16);
      LAS unsigned char* rp1 = B + q * 256 + ((n ^ (4 + q)) * 16);
#pragma unroll
      for (int blk = 0; blk < 4; ++blk) {
#pragma unroll
        for (int i = 0; i < 8; ++i) *(LAS v4u*)(wp + (((2 * i + (lane >> 5)) ^ fw) * 16)) = xr[blk][i];
        LGKM0();
#pragma unroll
        for (int k8 = 0; k8 < 8; ++k8) {
          const v4u f = *(LAS v4u*)(((k8 & 1) ? rp1 : rp0) + k8 * 1024);
          if (blk < 2) xa[(blk & 1) * 8 + k8] = f; else xb[(blk & 1) * 8 + k8] = f;
#pragma unroll
          for (int e = 0; e < 4; ++e) {
            if (blk < 2) { s1a = dot2(f[e], 0x3f803f80u, s1a); s2a = dot2(f[e], f[e], s2a); }
            else { s1b = dot2(f[e], 0x3f803f80u, s1b); s2b = dot2(f[e], f[e], s2b); }
          }
        }
        PIN(s1a); PIN(s2a); PIN(s1b); PIN(s2b);
        LGKM0();
      }
    }
    STAMP(2);
    const int r4 = n >> 2, c4 = n & 3;
    unsigned trb[2];
#pragma unroll
    for (int ct = 0; ct < 2; ++ct)
      trb[ct] = (unsigned)((q >> 1) * 1024 + ((q & 1) * 8 + r4) * 64 + (2 * (ct ^ (q & 1)) + (c4 >> 1)) * 16 + (c4 & 1) * 8);
    const v4u ones = {0x3f803f80u, 0x3f803f80u, 0x3f803f80u, 0x3f803f80u};
    {
#pragma unroll
      for (int i = 0; i < 4; ++i) *(LAS v4u*)(B + i * 1024 + l16) = pt[i];
      LGKM0();
      f32x4 c1[2] = {z4, z4};
#pragma unroll
      for (int u = 0; u < 2; ++u)
#pragma unroll
        for (int ct = 0; ct < 2; ++ct) {
          const v4u f = tr_frag(B + u * 2048 + trb[ct]);
          const int gct = 2 * pp + ct, ks = pks + u;
          unsigned dst;
          if (pkind == 0) dst = OFF_W1 + (unsigned)((gct / 7) * 112 + (gct % 7) * 16 + ks) * 1024u;
          else if (pkind == 1) { const int p = ks / 7, t = ks % 7; dst = OFF_W2 + (unsigned)(((gct >> 2) * 4 + (p >> 1)) * 56 + (2 * t + (p & 1)) * 4 + (gct & 3)) * 1024u; }
          else dst = OFF_WO + (unsigned)((gct >> 2) * 64 + (gct & 3) * 16 + ks) * 1024u;
          stb1(rs, dst + l16, f);
          c1[ct] = mfma(f, ones, c1[ct]);
        }
      if (pkind == 0 && n == 0) {
        const int part = (b & 1) * 4 + w;
#pragma unroll
        for (int ct = 0; ct < 2; ++ct)
          stb1(rs, OFF_CS1 + (unsigned)(part * 1792 + 32 * pp + 16 * ct + 4 * q) * 4u, __builtin_bit_cast(v4u, c1[ct]));
      }
    }
    Stats st;
    finish_stats(sum_q(s1a), sum_q(s2a), st.a[0], st.b[0]);
    finish_stats(sum_q(s1b), sum_q(s2b), st.a[1], st.b[1]);
    if (q == 0) {
      *(LAS f32x2*)(L + L_STX + w * 256 + n * 8) = f32x2{st.a[0], st.b[0]};
      *(LAS f32x2*)(L + L_STX + w * 256 + 128 + n * 8) = f32x2{st.a[1], st.b[1]};
    }
    STAMP(3);
    DRAIN();
    BAR();
    STAMP(4);
    {
      f32x4 cs[6] = {z4, z4, z4, z4, z4, z4};
#pragma unroll
      for (int i = 0; i < 4; ++i)
#pragma unroll
        for (int seg = 0; seg < 3; ++seg) {
          LAS unsigned char* blk = L + seg * 32768 + (4 * w + i) * 2048;
          const v4u f0 = tr_frag(blk + trb[0]), f1 = tr_frag(blk + trb[1]);
          const int t0 = seg == 0 ? 4 : seg - 1, t1 = seg == 0 ? 5 : seg + 1;
          if (seg == 0) { cs[4] = mfma(f0, ones, cs[4]); cs[5] = mfma(f1, ones, cs[5]); }
          else { cs[seg - 1] = mfma(ones, f0, cs[seg - 1]); cs[seg + 1] = mfma(ones, f1, cs[seg + 1]); }
          *(LAS v4u*)(blk + l16) = f0;
          *(LAS v4u*)(blk + 1024 + l16) = f1;
          if (4 * w + i == g) {
            stb1(rs, OFF_WQKV + (unsigned)(j * 96 + t0 * 16 + g) * 1024u + l16, f0);
            stb1(rs, OFF_WQKV + (unsigned)(j * 96 + t1 * 16 + g) * 1024u + l16, f1);
          }
        }
      LAS float* csw = (LAS float*)(L + L_CSP) + w * 96;
#pragma unroll
      for (int t = 4; t < 6; ++t) if (n == 0) *(LAS f32x4*)(csw + t * 16 + 4 * q) = cs[t];
#pragma unroll
      for (int t = 0; t < 4; ++t) if (q == 0) csw[t * 16 + n] = cs[t][0];
    }
    BAR();
    if (tid < 96) {
      LAS float* c = (LAS float*)(L + L_CSP);
      const float s = (c[tid] + c[96 + tid]) + (c[192 + tid] + c[288 + tid]);
      ((LAS float*)(L + L_CSQ))[tid] = s;
      if (g == 0) __hip_atomic_store((GAS float*)(ws + OFF_CSQ) + j * 96 + tid, s, RLX_AGENT);
    }
    DRAIN();
    BAR();
    if (tid == 0) __hip_atomic_store(fb + FO_PRE / 4 + b, epoch, RLX_AGENT);
    f32x4 acc[6][2];
    {
      constexpr int FD = 6, NF = 96;
      auto addr = [&](int i) -> unsigned {
        const int t = i % 6, ks = i / 6;
        const int seg = t < 4 ? 1 + (t & 1) : 0, ct = t < 4 ? (t >> 1) : (t - 4);
        return (unsigned)(seg * 32768 + ks * 2048 + ct * 1024);
      };
      v4u wf[FD];
#pragma unroll
      for (int i = 0; i < FD; ++i) wf[i] = ldsf(lb, addr(i));
      SCHED();
      static_for<NF>([&](auto I) {
        constexpr int i = I, t = i % 6, ks = i / 6;
        const v4u f = wf[i % FD];
        if constexpr (i + FD < NF) wf[i % FD] = ldsf(lb, addr(i + FD));
        if constexpr (t < 4) mfma2<ks == 0>(acc[t][0], acc[t][1], xa[ks], f, xb[ks], f);
        else mfma2<ks == 0>(acc[t][0], acc[t][1], f, xa[ks], f, xb[ks]);
        SCHED();
      });
    }
    MFMA_FENCE();
#pragma unroll
    for (int t = 0; t < 6; ++t) { PINV(acc[t][0]); PINV(acc[t][1]); }
    STAMP(5);
    f32x4 kva, kvb, za, zb;
    kv_epilogue(acc, L + L_STX + w * 256, (LAS float*)(L + L_CSQ), lane, kva, kvb, za, zb);
    v4u pq0[2];
    q_epilogue(acc, st, (LAS float*)(L + L_CSQ), lane, pq0);
    const int cu = g * 16 + j;
#pragma unroll
    for (int rt = 0; rt < 2; ++rt) stb1(rs, OFF_PQ + (unsigned)((cu * 4 + w) * 2 + rt) * 1024u + l16, pq0[rt]);
    if (w == 0) {
      for (unsigned sp = 0;;) {
        const v4u v = ldb1(rs, OFF_FLAGS + FO_PRE + l16);
        if (__all(v.x == epoch && v.y == epoch && v.z == epoch && v.w == epoch)) break;
        __builtin_amdgcn_s_sleep(4);
        if (spin_fail(sp, fb, epoch)) break;
      }
    }
    kv_publish(L, L_KVX, rs, OFF_KVP + (unsigned)(j * 16 + g) * KVREC, lane, w, kva, kvb, za, zb);
    STAMP(6);
    publish(fb, FC_KV + 2 * j, tid);
    STAMP(7);
  }

  const int g = gm, j = jm;
  const int cu = g * 16 + j;
  const int myrt = 8 * g + 4 * hh + w;
  const int rt0 = 8 * g + 2 * w;
  v4u pq[2];
  v2u res[4];
  dma_w123<64>(ws, OFF_WO + cs * 65536u, lbase + WOU[0] * LU, w, lane0);
  dma_w123<32>(ws, OFF_W1 + j * 114688u, lbase + W1U[0][0] * LU, w, lane0);
  static_for<14>([&](auto I) { ldw2(I, (unsigned)lane0 * 16u); });
  {
    FRESH();
    const GAS v2u* xp = (const GAS v2u*)(x + (size_t)(16 * myrt + n) * 512 + 64 * cs + 4 * q);
#pragma unroll
    for (int c = 0; c < 4; ++c) res[c] = xp[c * 4];
    if (tid < 112) {
      float csv = 0.f;
#pragma unroll
      for (int i = 0; i < 8; ++i) csv += __hip_atomic_load((GAS float*)(ws + OFF_CS1) + i * 1792 + 112 * j + tid, RLX_AGENT);
      ((LAS float*)(L + L_CS1))[tid] = csv;
    } else if (tid >= 128 && tid < 224) {
      ((LAS float*)(L + L_CSQ))[tid - 128] = __hip_atomic_load((GAS float*)(ws + OFF_CSQ) + j * 96 + tid - 128, RLX_AGENT);
    }
  }
  using std::integral_constant;
#define IC(x) integral_constant<int, (x)>{}
#define VMW(n) asm volatile("s_waitcnt vmcnt(%0)" :: "i"(n) : "memory")

  static_for<2>([&](auto LL) {
    constexpr int l = LL;
    constexpr int SB = 10 + l * 24;
    if (w == 0) poll_ge(fb, FC_KV + (l * 16 + j) * 2, 16u, epoch);
    BAR();
    STAMP(SB + 0);
    {
      FRESH();
      if constexpr (l == 0) {
#pragma unroll
        for (int rt = 0; rt < 2; ++rt) pq[rt] = ldb1(rs, OFF_PQ + (unsigned)((cu * 4 + w) * 2 + rt) * 1024u + l16);
      }
      const unsigned rb = OFF_KVP + (unsigned)((l * 16 + j) * 16) * KVREC;
      if (w < 3) {
        const unsigned o = rb + (w < 2 ? (unsigned)w * 1024u + l16 : 2048u + (unsigned)(lane & 7) * 16u);
        v4u a[16];
#pragma unroll
        for (int i = 0; i < 16; ++i) a[i] = ldb1(rs, o + i * KVREC);
        SCHED();
        f32x4 s[4];
#pragma unroll
        for (int i = 0; i < 4; ++i)
          s[i] = (__builtin_bit_cast(f32x4, a[4 * i]) + __builtin_bit_cast(f32x4, a[4 * i + 1])) + (__builtin_bit_cast(f32x4, a[4 * i + 2]) + __builtin_bit_cast(f32x4, a[4 * i + 3]));
        const f32x4 t = (s[0] + s[1]) + (s[2] + s[3]);
        const v2u pk = v2u{pack2(t[0], t[1]), pack2(t[2], t[3])};
        if (w < 2) *(LAS v2u*)(L + L_KVX + w * 512 + lane * 8) = pk;
        else if (lane < 8) *(LAS v2u*)(L + L_KVX + 1024 + lane * 8) = pk;
      }
      BAR();
      const v2u ka = *(LAS v2u*)(L + L_KVX + lane * 8), kb = *(LAS v2u*)(L + L_KVX + 512 + lane * 8);
      const v2u zz = *(LAS v2u*)(L + L_KVX + 1024 + ((n & 1) * 4 + q) * 8);
      const v4u Aa = {ka.x, ka.y, 0u, 0u};
      const v4u Ab = {0u, 0u, kb.x, kb.y};
      const bool za_ = (n & 3) == 0, zb_ = (n & 3) == 1;
      const v4u Az = {za_ ? zz.x : 0u, za_ ? zz.y : 0u, zb_ ? zz.x : 0u, zb_ ? zz.y : 0u};
#pragma unroll
      for (int rt = 0; rt < 2; ++rt) {
        const f32x4 na = mfma(Aa, pq[rt], z4), nb = mfma(Ab, pq[rt], z4), dn = mfma(Az, pq[rt], z4);
        const float ra = __builtin_amdgcn_rcpf(dn[0] + 1e-6f), rb2 = __builtin_amdgcn_rcpf(dn[1] + 1e-6f);
        const v4u o = pair16(pack2(na[0] * ra, na[1] * ra), pack2(na[2] * ra, na[3] * ra), pack2(nb[0] * rb2, nb[1] * rb2), pack2(nb[2] * rb2, nb[3] * rb2));
        const unsigned kg = (q & 1) * 2 + (q >> 1);
        stb1(rs, OFF_O + l * XF_BYTES + (unsigned)((rt0 + rt) * 16 + j) * 1024u + (kg * 16 + n) * 16, o);
      }
      STAMP(SB + 1);
      DRAIN();
      arrive(fb, FC_O + (l * 16 + g) * 4 + w, 0, lane);
      BAR();
      STAMP(SB + 2);
      if constexpr (l == 0) {
        static_for<12>([&](auto K) {
          const unsigned pc = (unsigned)((K & 3) * 4 + w) * 1024u;
          dma16(wsp + OFF_W1 + jo * 114688u + (2 + (K >> 2)) * LU + pc, l16, lbase + W1U[0][2 + (K >> 2)] * LU + pc);
        });
      } else {
        static_for<8>([&](auto K) {
          const unsigned pc = (unsigned)((K & 3) * 4 + w) * 1024u;
          dma16(wsp + OFF_W1 + jo * 114688u + (3 + (K >> 2)) * LU + pc, l16, lbase + W1U[1][3 + (K >> 2)] * LU + pc);
        });
      }
    }
    poll_ge(fb, FC_O + (l * 16 + g) * 4 + ((4 * hh + w) >> 1), 16u, epoch);
    STAMP(SB + 3);
    {
      FRESH();
      v4u a[16];
      const unsigned ab = OFF_O + l * XF_BYTES + (unsigned)(myrt * 16) * 1024u + l16;
      constexpr int AD = 8;
#pragma unroll
      for (int ks = 0; ks < AD; ++ks) a[ks] = ldb1(rs, ab + ks * 1024);
      f32x4 acc[4];
      constexpr int FD = 8;
      v4u wf[FD];
#pragma unroll
      for (int i = 0; i < FD; ++i) wf[i] = ldsf(lb, (WOU[l] + (i & 3)) * LU + (i >> 2) * 1024);
      SCHED();
      static_for<64>([&](auto I) {
        constexpr int i = I;
        const v4u f = wf[i % FD];
        if constexpr (i + FD < 64) wf[i % FD] = ldsf(lb, (WOU[l] + ((i + FD) & 3)) * LU + ((i + FD) >> 2) * 1024);
        mfma1<(i < 4)>(acc[i & 3], f, a[i >> 2]);
        if constexpr ((i & 3) == 1 && (i >> 2) + AD < 16) a[(i >> 2) + AD] = ldb1(rs, ab + ((i >> 2) + AD) * 1024);
        SCHED();
      });
      MFMA_FENCE();
      PINV(acc[0]); PINV(acc[1]); PINV(acc[2]); PINV(acc[3]);
      res_update(res, acc);
      store_x_tile(rs, OFF_X1 + l * XF_BYTES, OFF_ST + l * ST_BYTES, res, myrt, cs, lane);
      STAMP(SB + 4);
      DRAIN();
      arrive(fb, FC_X1 + (l * 16 + g) * 8 + 4 * hh + w, 0, lane);
      BAR();
      STAMP(SB + 5);
      static_for<8>([&](auto K) {
        const unsigned pc = (unsigned)((K & 3) * 4 + w) * 1024u;
        dma16(wsp + OFF_W1 + jo * 114688u + (5 + (K >> 2)) * LU + pc, l16, lbase + W1U[l][5 + (K >> 2)] * LU + pc);
      });
      if constexpr (l == 0) static_for<14>([&](auto I) { ldw2(IC(14 + I), l16); });
    }

    poll_ge(fb, FC_X1 + (l * 16 + g) * 8 + 2 * w, 8u, epoch);
    poll_ge(fb, FC_X1 + (l * 16 + g) * 8 + 2 * w + 1, 8u, epoch);
    STAMP(SB + 6);
    {
      FRESH();
      const int hline_out = FC_H + ((l * 16 + g) * 2 + (w >> 1)) * 4 + (j >> 2), hline_in = FC_H + ((l * 16 + g) * 2 + hh) * 4 + w;
      constexpr int WIN = 16;
      v4u a[WIN];
      unsigned hb[4];
#pragma unroll
      for (int i4 = 0; i4 < 4; ++i4) hb[i4] = OFF_H + l * HF_BYTES + (unsigned)((8 * g + 4 * hh + ((w + i4) & 3)) * 56 + 14 * w) * 1024u + l16;
      auto hoff = [](int f) -> unsigned { return (unsigned)(((f >> 2) & 1) * 7 + (f >> 3)) * 1024u; };
      {
        const unsigned ab = OFF_X1 + l * XF_BYTES + (unsigned)(rt0 * 16) * 1024u + l16;
        v4u xa[16], xb[16], sp[2];
        constexpr int AD = 6, FD = 6, NF = 112;
        load_stats(rs, OFF_ST + l * ST_BYTES, rt0, lane, sp);
#pragma unroll
        for (int ks = 0; ks < AD; ++ks) { xa[ks] = ldb1(rs, ab + ks * 1024); xb[ks] = ldb1(rs, ab + (16 + ks) * 1024); }
        VMW(2 * AD + 2);
        BAR();
        constexpr auto ft = [](int i) { return i < 64 ? (i & 3) : 4 + (i - 64) % 3; };
        constexpr auto fk = [](int i) { return i < 64 ? (i >> 2) : (i - 64) / 3; };
        auto faddr = [&](int i) -> unsigned { return (unsigned)(W1U[l][ft(i)] * LU + fk(i) * 1024); };
        f32x4 acc[7][2];
        v4u wf[FD];
#pragma unroll
        for (int i = 0; i < FD; ++i) wf[i] = ldsf(lb, faddr(i));
        SCHED();
        float sa[2], sb[2];
        const unsigned hst = OFF_H + l * HF_BYTES + ((unsigned)(rt0 + (q & 1)) * 56u + (unsigned)(j >> 1) * 7u) * 1024u + ((2 * (j & 1) + (q >> 1)) * 16 + n) * 16;
        auto epi = [&](auto T) {
          constexpr int t = T;
          const f32x4 c = *(LAS f32x4*)(L + L_CS1 + (t * 16 + 4 * q) * 4);
          float y[8];
#pragma unroll
          for (int i = 0; i < 8; ++i) {
            const float v = fmaf(acc[t][i >> 2][i & 3], sa[i >> 2], sb[i >> 2] * c[i & 3]);
            y[i] = v * __builtin_amdgcn_rcpf(fmaf(__builtin_amdgcn_exp2f(-v), LOG2E, LOG2E));
          }
          stb1(rs, hst + t * 1024, pair16(pack2(y[0], y[1]), pack2(y[2], y[3]), pack2(y[4], y[5]), pack2(y[6], y[7])));
        };
        auto kloop = [&](auto LO, auto N) {
          static_for<N>([&](auto I) {
            constexpr int i = LO + I, t = ft(i), ks = fk(i);
            const v4u f = wf[i % FD];
            if constexpr (i + FD < NF) wf[i % FD] = ldsf(lb, faddr(i + FD));
            mfma2<ks == 0>(acc[t][0], acc[t][1], f, xa[ks], f, xb[ks]);
            if constexpr (i < 64 && t == 1 && ks + AD < 16) { xa[ks + AD] = ldb1(rs, ab + (ks + AD) * 1024); xb[ks + AD] = ldb1(rs, ab + (16 + ks + AD) * 1024); }
            if constexpr (i == 94) { VMW(0); arrive(fb, hline_out, 0, lane); }
            if constexpr (l == 0 && i >= 96 && i < 110) { ldw2(IC(28 + 2 * (i - 96)), l16); ldw2(IC(29 + 2 * (i - 96)), l16); }
            if constexpr (i == 0) STAMP(SB + 16);
            SCHED();
          });
          MFMA_FENCE();
          static_for<ft(LO + N - 1) - ft(LO) + 1>([&](auto T) { PINV(acc[ft(LO) + T][0]); PINV(acc[ft(LO) + T][1]); });
        };
        kloop(IC(0), IC(64));
        {
          Stats st;
          make_stats(sp, st);
          sa[0] = st.a[0] * LOG2E; sa[1] = st.a[1] * LOG2E; sb[0] = st.b[0] * LOG2E; sb[1] = st.b[1] * LOG2E;
        }
        static_for<4>([&](auto T) { epi(T); });
        STAMP(SB + 17);
        SCHED();
        kloop(IC(64), IC(48));
        STAMP(SB + 18);
        poll_dw(fb, hline_in, 0, 8u, epoch);
#pragma unroll
        for (int f = 0; f < WIN; ++f) a[f] = ldb1(rs, hb[f & 3] + hoff(f));
        static_for<3>([&](auto T) { epi(IC(4 + T)); });
      }
      STAMP(SB + 7);
      BAR();
      f32x4 acc2[4][4];
      static_for<56>([&](auto F) {
        constexpr int f = F, wi = 2 * (f >> 3) + ((f >> 2) & 1);
        const v4u cur = a[f % WIN];
        if constexpr (f == 10) { VMW(10); arrive(fb, hline_out, 1, lane); }
        if constexpr (f + WIN < 56) {
          if constexpr (f + WIN == 32) poll_dw(fb, hline_in, 1, 8u, epoch);
          a[f % WIN] = ldb1(rs, hb[(f + WIN) & 3] + hoff(f + WIN));
        }
        mfma4a<(f < 4)>(acc2[f & 3], w2[wi][0], w2[wi][1], w2[wi][2], w2[wi][3], cur);
        if constexpr (l == 0 && f >= 20 && f < 44) {
          const unsigned o = (unsigned)((f - 20) * 4 + w) * 1024u;
          dma16(wsp + OFF_WQKV + jo * 98304u + o, l16, lbase + QU * LU + o);
        }
        SCHED();
      });
      MFMA_FENCE();
#pragma unroll
      for (int i4 = 0; i4 < 4; ++i4)
#pragma unroll
        for (int c = 0; c < 4; ++c) PINV(acc2[i4][c]);
      STAMP(SB + 9);
#pragma unroll
      for (int i4 = 1; i4 < 4; ++i4) {
        const unsigned d = (w + i4) & 3;
#pragma unroll
        for (int c = 0; c < 4; ++c) *(LAS f32x4*)(L + REDU[l][i4 - 1] * LU + (d * 4 + c) * 1024 + l16) = acc2[i4][c];
      }
      BAR();
#pragma unroll
      for (int sl = 0; sl < 3; ++sl)
#pragma unroll
        for (int c = 0; c < 4; ++c) acc2[0][c] += *(LAS f32x4*)(L + REDU[l][sl] * LU + (w * 4 + c) * 1024 + l16);
      res_update(res, acc2[0]);
      if constexpr (l == 0) {
        store_x_tile(rs, OFF_X2, OFF_ST + 2 * ST_BYTES, res, myrt, cs, lane);
      } else {
#pragma unroll
        for (int c2 = 0; c2 < 2; ++c2) {
          const v4u o = pair16(res[2 * c2].x, res[2 * c2].y, res[2 * c2 + 1].x, res[2 * c2 + 1].y);
          *(GAS v4u*)(out + (size_t)(16 * myrt + n) * 512 + 64 * cs + 16 * (2 * c2 + (q & 1)) + 8 * (q >> 1)) = o;
        }
      }
      STAMP(SB + 11);
      if constexpr (l == 0) {
        DRAIN();
        arrive(fb, FC_X2 + g * 8 + 4 * hh + w, 0, lane);
      }
    }
    if constexpr (l == 0) {
      poll_ge(fb, FC_X2 + g * 8 + 2 * w, 8u, epoch);
      poll_ge(fb, FC_X2 + g * 8 + 2 * w + 1, 8u, epoch);
      STAMP(SB + 13);
      FRESH();
      const unsigned ab = OFF_X2 + (unsigned)(rt0 * 16) * 1024u + l16;
      v4u sp[2];
      load_stats(rs, OFF_ST + 2 * ST_BYTES, rt0, lane, sp);
      BAR();
      f32x4 acc[6][2];
      Stats st;
      gemm_k<6, 4, 6, 6>(rs, ab, lb, acc, [](int t, int ks) -> unsigned { return (unsigned)((QU + t) * LU + ks * 1024); },
        [&](auto I) {
          constexpr int i = I;
          if constexpr (i == 72) {
            make_stats(sp, st);
            if (q == 0) {
              *(LAS f32x2*)(L + L_STX + w * 256 + n * 8) = f32x2{st.a[0], st.b[0]};
              *(LAS f32x2*)(L + L_STX + w * 256 + 128 + n * 8) = f32x2{st.a[1], st.b[1]};
            }
          }
          if constexpr ((i & 7) == 4) {
            constexpr int k = i >> 3;
            const unsigned pc = (unsigned)((k & 3) * 4 + w) * 1024u;
            dma16(wsp + OFF_W1 + jo * 114688u + (k >> 2) * LU + pc, l16, lbase + W1U[1][k >> 2] * LU + pc);
          }
        });
      f32x4 kva, kvb, za, zb;
      kv_epilogue(acc, L + L_STX + w * 256, (LAS float*)(L + L_CSQ), lane, kva, kvb, za, zb);
      kv_publish(L, L_KVX, rs, OFF_KVP + (unsigned)((16 + j) * 16 + g) * KVREC, lane, w, kva, kvb, za, zb);
      STAMP(SB + 14);
      publish(fb, FC_KV + (16 + j) * 2, tid);
      STAMP(SB + 15);
      dma_w123<64>(ws, OFF_WO + cs * 65536u, lbase + WOU[1] * LU, w, lane0);
      q_epilogue(acc, st, (LAS float*)(L + L_CSQ), lane, pq);
      if (b == 0 && tid == 0) __hip_atomic_store(fb + FO_EPOCH / 4, epoch, RLX_AGENT);
    }
  });
  STAMP_FLUSH(0, 64);
}

}

extern "C" void kernel_launch(void* const* d_in, const int* in_sizes, int n_in, void* d_out, int out_size, void* d_ws, size_t ws_size,
                              hipStream_t stream) {
  const u16* x = (const u16*)d_in[0];
  const u16* wqkv = (const u16*)d_in[1];
  const u16* wo = (const u16*)d_in[2];
  const u16* w1 = (const u16*)d_in[4];
  const u16* w2 = (const u16*)d_in[5];
  (void)hipFuncSetAttribute((const void*)kmain, hipFuncAttributeMaxDynamicSharedMemorySize, K2_LDS);
  kmain<<<256, 256, K2_LDS, stream>>>(x, wqkv, wo, w1, w2, (u16*)d_out, (unsigned char*)d_ws);
}
```

```cpp
#include <hip/hip_runtime.h>
#include <stdint.h>
#include <utility>

namespace {

typedef unsigned v4u __attribute__((ext_vector_type(4)));
typedef unsigned v2u __attribute__((ext_vector_type(2)));
typedef float f32x4 __attribute__((ext_vector_type(4)));
typedef float f32x2 __attribute__((ext_vector_type(2)));
typedef __bf16 bf2 __attribute__((ext_vector_type(2)));
typedef __bf16 bf8 __attribute__((ext_vector_type(8)));
typedef short s4v __attribute__((ext_vector_type(4)));
typedef unsigned short u16;
#define DI __device__ __forceinline__
#define GAS __attribute__((address_space(1)))
#define LAS __attribute__((address_space(3)))
typedef GAS unsigned gu32;
#define RLX_AGENT __ATOMIC_RELAXED, __HIP_MEMORY_SCOPE_AGENT
typedef __amdgpu_buffer_rsrc_t rsrc_t;

constexpr unsigned OFF_FLAGS = 0;
constexpr unsigned OFF_WQKV = 124928;
constexpr unsigned OFF_WO = OFF_WQKV + 1536u * 1024u;
constexpr unsigned OFF_W1 = OFF_WO + 512u * 1024u;
constexpr unsigned OFF_W2 = OFF_W1 + 1792u * 1024u;
constexpr unsigned OFF_CS1 = OFF_W2 + 1792u * 1024u;
constexpr unsigned OFF_CSQ = OFF_CS1 + 8u * 1792u * 4u;
constexpr unsigned OFF_PQ = OFF_CSQ + 16u * 96u * 4u;
constexpr unsigned KVREC = 2304;
constexpr unsigned OFF_KVP = OFF_PQ + 256u * 8192u;
constexpr unsigned OFF_ST = OFF_KVP + 2u * 256u * KVREC;
constexpr unsigned ST_BYTES = 128u * 1024u;
constexpr unsigned OFF_O = OFF_ST + 3u * ST_BYTES;
constexpr unsigned XF_BYTES = 128u * 16u * 1024u;
constexpr unsigned OFF_X1 = OFF_O + 2u * XF_BYTES;
constexpr unsigned OFF_X2 = OFF_X1 + 2u * XF_BYTES;
constexpr unsigned HF_BYTES = 128u * 56u * 1024u;
constexpr unsigned OFF_H = OFF_X2 + XF_BYTES;
constexpr unsigned WS_BYTES = OFF_H + 2u * HF_BYTES;
static_assert(OFF_O % 256 == 0 && OFF_KVP % 256 == 0 && OFF_PQ % 256 == 0 && OFF_ST % 256 == 0, "align");

constexpr unsigned FO_PRE = 0, FO_EPOCH = 1024, FO_TMO = 1088, FO_CNT = 2048;
constexpr int FC_O = 0;
constexpr int FC_X1 = 128;
constexpr int FC_H = 704;
constexpr int FC_X2 = 512;
constexpr int FC_KV = 640;
constexpr unsigned FLAG_BYTES = 2048 + 256 * 480;
static_assert(FO_CNT + (FC_H + 256) * 128 <= FLAG_BYTES, "flags");
constexpr unsigned SPIN_CAP = 1u << 17;

constexpr int AUX_SC1 = 16;

constexpr unsigned LU = 16384;
constexpr unsigned L_SCR = 9 * LU;
constexpr unsigned L_CS1 = L_SCR;
constexpr unsigned L_CSQ = L_SCR + 512;
constexpr unsigned L_KVX = L_SCR + 1024;
constexpr unsigned L_CSP = L_SCR + 9728;
constexpr unsigned L_STX = L_SCR + 11264;
constexpr unsigned L_XB = 6 * LU;
constexpr int W1U[2][7] = {{4, 5, 6, 7, 8, 0, 1}, {0, 1, 8, 6, 7, 2, 3}};
constexpr int WOU[2] = {0, 2};
constexpr int QU = 2;
constexpr int REDU[2][3] = {{0, 1, 8}, {4, 5, 6}};
constexpr unsigned K2_LDS = 10 * LU;

DI f32x4 mfma(v4u a, v4u b, f32x4 c) {
  return __builtin_amdgcn_mfma_f32_16x16x32_bf16(__builtin_bit_cast(bf8, a), __builtin_bit_cast(bf8, b), c, 0, 0, 0);
}
template <bool FIRST> DI void mfma2(f32x4& c0, f32x4& c1, v4u a0, v4u b0, v4u a1, v4u b1) {
  if constexpr (FIRST)
    asm volatile("v_mfma_f32_16x16x32_bf16 %0, %2, %3, 0\n\tv_mfma_f32_16x16x32_bf16 %1, %4, %5, 0"
                 : "=&v"(c0), "=&v"(c1) : "v"(a0), "v"(b0), "v"(a1), "v"(b1));
  else
    asm volatile("v_mfma_f32_16x16x32_bf16 %0, %2, %3, %0\n\tv_mfma_f32_16x16x32_bf16 %1, %4, %5, %1"
                 : "+v"(c0), "+v"(c1) : "v"(a0), "v"(b0), "v"(a1), "v"(b1));
}
template <bool FIRST> DI void mfma1(f32x4& c0, v4u a0, v4u b0) {
  if constexpr (FIRST) asm volatile("v_mfma_f32_16x16x32_bf16 %0, %1, %2, 0" : "=&v"(c0) : "v"(a0), "v"(b0));
  else asm volatile("v_mfma_f32_16x16x32_bf16 %0, %1, %2, %0" : "+v"(c0) : "v"(a0), "v"(b0));
}
template <bool FIRST> DI void mfma4a(f32x4 (&c)[4], v4u w0, v4u w1, v4u w2, v4u w3, v4u b) {
  if constexpr (FIRST)
    asm volatile("s_nop 1\n\tv_mfma_f32_16x16x32_bf16 %0, %4, %8, 0\n\tv_mfma_f32_16x16x32_bf16 %1, %5, %8, 0\n\t"
                 "v_mfma_f32_16x16x32_bf16 %2, %6, %8, 0\n\tv_mfma_f32_16x16x32_bf16 %3, %7, %8, 0"
                 : "=&v"(c[0]), "=&v"(c[1]), "=&v"(c[2]), "=&v"(c[3]) : "a"(w0), "a"(w1), "a"(w2), "a"(w3), "v"(b));
  else
    asm volatile("s_nop 1\n\tv_mfma_f32_16x16x32_bf16 %0, %4, %8, %0\n\tv_mfma_f32_16x16x32_bf16 %1, %5, %8, %1\n\t"
                 "v_mfma_f32_16x16x32_bf16 %2, %6, %8, %2\n\tv_mfma_f32_16x16x32_bf16 %3, %7, %8, %3"
                 : "+v"(c[0]), "+v"(c[1]), "+v"(c[2]), "+v"(c[3]) : "a"(w0), "a"(w1), "a"(w2), "a"(w3), "v"(b));
}
#define MFMA_FENCE() asm volatile("s_nop 15\n\ts_nop 3" ::: "memory")
DI void pinv(f32x4& x) { asm volatile("" : "+v"(x)); }
#define PINV(x) pinv(x)
DI unsigned pack2(float a, float b) {
  f32x2 v = {a, b};
  return __builtin_bit_cast(unsigned, __builtin_convertvector(v, bf2));
}
DI float bflo(unsigned u) { return __builtin_bit_cast(float, u << 16); }
DI float bfhi(unsigned u) { return __builtin_bit_cast(float, u & 0xffff0000u); }
DI float dot2(unsigned a, unsigned b, float c) {
  return __builtin_amdgcn_fdot2_f32_bf16(__builtin_bit_cast(bf2, a), __builtin_bit_cast(bf2, b), c, false);
}
constexpr float LOG2E = 1.4426950408889634f;
DI float phi(float t) { return __builtin_amdgcn_exp2f(fminf(t, 0.f) * LOG2E) + (fmaxf(t, 0.f) + 1e-6f); }
DI v4u ldb1(rsrc_t rs, unsigned off) { return __builtin_amdgcn_raw_buffer_load_b128(rs, off, 0, AUX_SC1); }
DI void stb1(rsrc_t rs, unsigned off, v4u v) { __builtin_amdgcn_raw_buffer_store_b128(v, rs, off, 0, AUX_SC1); }
DI void stb1_64(rsrc_t rs, unsigned off, v2u v) { __builtin_amdgcn_raw_buffer_store_b64(v, rs, off, 0, AUX_SC1); }
#define DRAIN() asm volatile("s_waitcnt vmcnt(0)" ::: "memory")
#define LGKM0() asm volatile("s_waitcnt lgkmcnt(0)" ::: "memory")
#define BAR() do { asm volatile("s_waitcnt lgkmcnt(0)" ::: "memory"); __builtin_amdgcn_s_barrier(); asm volatile("" ::: "memory"); } while (0)
#define SCHED() __builtin_amdgcn_sched_barrier(0)
#define PIN(x) asm volatile("" : "+v"(x))


template <int... I, typename F> DI void static_for_impl(std::integer_sequence<int, I...>, F&& f) { (f(std::integral_constant<int, I>{}), ...); }
template <int N, typename F> DI void static_for(F&& f) { static_for_impl(std::make_integer_sequence<int, N>{}, f); }

DI void dma16(const void* sbase, unsigned voff, unsigned lds_dst) {
  unsigned keep;
  asm volatile("s_mov_b32 %0, m0\n\ts_mov_b32 m0, %3\n\ts_nop 3\n\tglobal_load_lds_dwordx4 %1, %2 sc1\n\ts_mov_b32 m0, %0"
               : "=&s"(keep) : "v"(voff), "s"(sbase), "s"(lds_dst) : "memory");
}
DI void dma16_in(const void* sbase, unsigned voff, unsigned lds_dst) {
  unsigned keep;
  asm volatile("s_mov_b32 %0, m0\n\ts_mov_b32 m0, %3\n\ts_nop 3\n\tglobal_load_lds_dwordx4 %1, %2\n\ts_mov_b32 m0, %0"
               : "=&s"(keep) : "v"(voff), "s"(sbase), "s"(lds_dst) : "memory");
}
DI v4u tr_frag(LAS unsigned char* p) {
  s4v lo = __builtin_amdgcn_ds_read_tr16_b64_v4i16((LAS s4v*)(p));
  s4v hi = __builtin_amdgcn_ds_read_tr16_b64_v4i16((LAS s4v*)(p + 256));
  v2u l2 = __builtin_bit_cast(v2u, lo), h2 = __builtin_bit_cast(v2u, hi);
  return v4u{l2.x, l2.y, h2.x, h2.y};
}
DI float sum16(float v) {
  const unsigned u = __builtin_bit_cast(unsigned, v);
  unsigned u2 = u; PIN(u2);
  auto r = __builtin_amdgcn_permlane16_swap(u, u2, false, false);
  const unsigned a = r[0], b = r[1];
  return __builtin_bit_cast(float, a) + __builtin_bit_cast(float, b);
}
DI float sum32(float v) {
  const unsigned u = __builtin_bit_cast(unsigned, v);
  unsigned u2 = u; PIN(u2);
  auto r = __builtin_amdgcn_permlane32_swap(u, u2, false, false);
  const unsigned a = r[0], b = r[1];
  return __builtin_bit_cast(float, a) + __builtin_bit_cast(float, b);
}
DI float sum_q(float v) { return sum32(sum16(v)); }
DI v4u pair16(unsigned a0, unsigned a1, unsigned b0, unsigned b1) {
  auto r0 = __builtin_amdgcn_permlane16_swap(a0, b0, false, false);
  auto r1 = __builtin_amdgcn_permlane16_swap(a1, b1, false, false);
  const unsigned x0 = r0[0], x1 = r1[0], x2 = r0[1], x3 = r1[1];
  return v4u{x0, x1, x2, x3};
}

DI bool spin_fail(unsigned& sp, gu32* fb, unsigned epoch) {
  if (++sp > SPIN_CAP) { __hip_atomic_store(fb + FO_TMO / 4, epoch, RLX_AGENT); return true; }
  return (sp & 255u) == 0u && __hip_atomic_load(fb + FO_TMO / 4, RLX_AGENT) == epoch;
}
DI void poll_ge(gu32* fb, int line, unsigned want, unsigned epoch) {
  gu32* wd = fb + (FO_CNT / 4) + line * 32;
  for (unsigned sp = 0;;) {
    if ((unsigned)__builtin_amdgcn_readfirstlane(__hip_atomic_load(wd, RLX_AGENT)) >= want) return;
    __builtin_amdgcn_s_sleep(1);
    if (spin_fail(sp, fb, epoch)) return;
  }
}
DI void poll_dw(gu32* fb, int line, int dw, unsigned want, unsigned epoch) {
  gu32* wd = fb + (FO_CNT / 4) + line * 32 + dw;
  for (unsigned sp = 0;;) {
    if ((unsigned)__builtin_amdgcn_readfirstlane(__hip_atomic_load(wd, RLX_AGENT)) >= want) return;
    __builtin_amdgcn_s_sleep(1);
    if (spin_fail(sp, fb, epoch)) return;
  }
}
DI void arrive(gu32* fb, int line, int dw, int lane) { if (lane == 0) __hip_atomic_fetch_add(fb + (FO_CNT / 4) + line * 32 + dw, 1u, RLX_AGENT); }

struct Stats { float a[2], b[2]; };
DI void finish_stats(float s1, float s2, float& a, float& b) {
  const float mean = s1 * (1.0f / 512.0f);
  const float var = fmaxf(s2 * (1.0f / 512.0f) - mean * mean, 0.f);
  a = __builtin_amdgcn_rsqf(var + 1e-5f);
  b = -mean * a;
}

DI void kv_epilogue(const f32x4 (&acc)[6][2], LAS unsigned char* stx, LAS float* csq, int lane, f32x4& kva, f32x4& kvb, f32x4& za, f32x4& zb) {
  const int q = lane >> 4, n = lane & 15;
  f32x4 ab[2][2];
#pragma unroll
  for (int rt = 0; rt < 2; ++rt) { ab[rt][0] = *(LAS f32x4*)(stx + rt * 128 + q * 32); ab[rt][1] = *(LAS f32x4*)(stx + rt * 128 + q * 32 + 16); }
  v4u PK[2], V[2];
#pragma unroll
  for (int h = 0; h < 2; ++h) {
    const float ck = csq[(2 * h) * 16 + n], cv = csq[(2 * h + 1) * 16 + n];
    unsigned pk[4], pv[4];
#pragma unroll
    for (int rt = 0; rt < 2; ++rt) {
      float a[4], b[4];
#pragma unroll
      for (int r = 0; r < 4; ++r) {
        const float sa = ab[rt][r >> 1][(r & 1) * 2], sb = ab[rt][r >> 1][(r & 1) * 2 + 1];
        a[r] = phi(fmaf(acc[2 * h][rt][r], sa, sb * ck));
        b[r] = fmaf(acc[2 * h + 1][rt][r], sa, sb * cv);
      }
      pk[2 * rt] = pack2(a[0], a[1]); pk[2 * rt + 1] = pack2(a[2], a[3]);
      pv[2 * rt] = pack2(b[0], b[1]); pv[2 * rt + 1] = pack2(b[2], b[3]);
    }
    PK[h] = v4u{pk[0], pk[1], pk[2], pk[3]};
    V[h] = v4u{pv[0], pv[1], pv[2], pv[3]};
  }
  const f32x4 z4 = {0, 0, 0, 0};
  const v4u ones = {0x3f803f80u, 0x3f803f80u, 0x3f803f80u, 0x3f803f80u};
  kva = mfma(PK[0], V[0], z4); kvb = mfma(PK[1], V[1], z4);
  za = mfma(PK[0], ones, z4); zb = mfma(PK[1], ones, z4);
}
DI void q_epilogue(const f32x4 (&acc)[6][2], const Stats& st, LAS float* csq, int lane, v4u (&pq)[2]) {
  const int q = lane >> 4;
#pragma unroll
  for (int rt = 0; rt < 2; ++rt) {
    unsigned p[4];
#pragma unroll
    for (int t = 0; t < 2; ++t) {
      const f32x4 c = *(LAS f32x4*)(csq + (4 + t) * 16 + 4 * q);
      float v[4];
#pragma unroll
      for (int r = 0; r < 4; ++r) v[r] = phi(fmaf(acc[4 + t][rt][r], st.a[rt], st.b[rt] * c[r]));
      p[2 * t] = pack2(v[0], v[1]); p[2 * t + 1] = pack2(v[2], v[3]);
    }
    pq[rt] = v4u{p[0], p[1], p[2], p[3]};
  }
}

DI void kv_publish(LAS unsigned char* L, unsigned kvx, rsrc_t rs, unsigned rec_off, int lane, int w, f32x4 kva, f32x4 kvb, f32x4 za, f32x4 zb) {
  LAS unsigned char* me = L + kvx + w * 2176;
  *(LAS f32x4*)(me + lane * 16) = kva;
  *(LAS f32x4*)(me + 1024 + lane * 16) = kvb;
  if ((lane & 15) == 0) {
    *(LAS f32x4*)(me + 2048 + (lane >> 4) * 16) = za;
    *(LAS f32x4*)(me + 2112 + (lane >> 4) * 16) = zb;
  }
  BAR();
  if (w < 2 || (w == 2 && lane < 8)) {
    const unsigned o = w * 1024 + lane * 16;
    f32x4 s = *(LAS f32x4*)(L + kvx + o);
#pragma unroll
    for (int ww = 1; ww < 4; ++ww) s += *(LAS f32x4*)(L + kvx + ww * 2176 + o);
    stb1(rs, rec_off + o, __builtin_bit_cast(v4u, s));
  }
}

struct LBase { LAS unsigned char* b[3]; };
DI v4u ldsf(const LBase& lb, unsigned off) { return *(LAS v4u*)(lb.b[off >> 16] + (off & 0xffffu)); }

DI void publish(gu32* fb, int line, int tid) {
  DRAIN();
  BAR();
  arrive(fb, line, 0, tid);
}
template <int NKB>
DI void dma_w123(const unsigned char* ws, unsigned src, unsigned ldst, int w, int lane) {
  if (w == 0) return;
  const unsigned char* p = ws + src + (unsigned)(w - 1) * 1024u;
  unsigned d = ldst + (unsigned)(w - 1) * 1024u;
#pragma unroll 1
  for (int i = w - 1; i < NKB; i += 3) {
    dma16(p, (unsigned)lane * 16u, d);
    p += 3072; d += 3072;
  }
}
template <int NT, int NTN, int AD, int FD, typename FA, typename HOOK>
DI void gemm_k(rsrc_t rs, unsigned ab, const LBase& lb, f32x4 (&acc)[NT][2], FA faddr, HOOK hook) {
  constexpr int R = AD + 1, NF = 16 * NT;
  v4u xa[R], xb[R];
#pragma unroll
  for (int ks = 0; ks < AD; ++ks) { xa[ks] = ldb1(rs, ab + ks * 1024); xb[ks] = ldb1(rs, ab + (16 + ks) * 1024); }
  v4u wf[FD];
#pragma unroll
  for (int i = 0; i < FD; ++i) wf[i] = ldsf(lb, faddr(i % NT, i / NT));
  SCHED();
  static_for<NF>([&](auto I) {
    constexpr int i = I, t = i % NT, ks = i / NT;
    const v4u f = wf[i % FD];
    if constexpr (i + FD < NF) wf[i % FD] = ldsf(lb, faddr((i + FD) % NT, (i + FD) / NT));
    if constexpr (t < NTN) mfma2<ks == 0>(acc[t][0], acc[t][1], xa[ks % R], f, xb[ks % R], f);
    else mfma2<ks == 0>(acc[t][0], acc[t][1], f, xa[ks % R], f, xb[ks % R]);
    if constexpr (t == 1 && ks + AD < 16) { xa[(ks + AD) % R] = ldb1(rs, ab + (ks + AD) * 1024); xb[(ks + AD) % R] = ldb1(rs, ab + (16 + ks + AD) * 1024); }
    hook(I);
    SCHED();
  });
  MFMA_FENCE();
#pragma unroll
  for (int t = 0; t < NT; ++t) { PINV(acc[t][0]); PINV(acc[t][1]); }
}
DI void res_update(v2u (&res)[4], const f32x4 (&acc)[4]) {
#pragma unroll
  for (int c = 0; c < 4; ++c) {
    const unsigned t0 = pack2(acc[c][0], acc[c][1]), t1 = pack2(acc[c][2], acc[c][3]);
    res[c].x = pack2(bflo(res[c].x) + bflo(t0), bfhi(res[c].x) + bfhi(t0));
    res[c].y = pack2(bflo(res[c].y) + bflo(t1), bfhi(res[c].y) + bfhi(t1));
  }
}
DI void store_x_tile(rsrc_t rs, unsigned xoff, unsigned stoff, const v2u (&res)[4], int myrt, int cs, int lane) {
  const int q = lane >> 4, n = lane & 15;
  const unsigned kg = (q & 1) * 2 + (q >> 1);
#pragma unroll
  for (int c2 = 0; c2 < 2; ++c2) {
    const v4u o = pair16(res[2 * c2].x, res[2 * c2].y, res[2 * c2 + 1].x, res[2 * c2 + 1].y);
    stb1(rs, xoff + (unsigned)(myrt * 16 + 2 * cs + c2) * 1024u + (kg * 16 + n) * 16, o);
  }
  float s1 = 0, s2 = 0;
#pragma unroll
  for (int c = 0; c < 4; ++c) {
    s1 = dot2(res[c].x, 0x3f803f80u, s1); s2 = dot2(res[c].x, res[c].x, s2);
    s1 = dot2(res[c].y, 0x3f803f80u, s1); s2 = dot2(res[c].y, res[c].y, s2);
  }
  s1 = sum_q(s1); s2 = sum_q(s2);
  if (q == 0) stb1_64(rs, stoff + (unsigned)myrt * 1024u + n * 64 + cs * 8, __builtin_bit_cast(v2u, f32x2{s1, s2}));
}
DI void load_stats(rsrc_t rs, unsigned stoff, int rt0, int lane, v4u (&sp)[2]) {
  const int q = lane >> 4, n = lane & 15;
#pragma unroll
  for (int rt = 0; rt < 2; ++rt) sp[rt] = ldb1(rs, stoff + (unsigned)(rt0 + rt) * 1024u + n * 64 + q * 16);
}
DI void make_stats(const v4u (&sp)[2], Stats& st) {
#pragma unroll
  for (int rt = 0; rt < 2; ++rt) {
    const f32x4 p = __builtin_bit_cast(f32x4, sp[rt]);
    finish_stats(sum_q(p[0] + p[2]), sum_q(p[1] + p[3]), st.a[rt], st.b[rt]);
  }
}

__global__ void __launch_bounds__(256, 1) kmain(const u16* __restrict__ x, const u16* __restrict__ wqkv, const u16* __restrict__ wo,
                                                const u16* __restrict__ w1, const u16* __restrict__ w2g, u16* __restrict__ out,
                                                unsigned char* __restrict__ ws) {
  extern __shared__ __attribute__((aligned(16))) unsigned char smem[];
  LAS unsigned char* L = (LAS unsigned char*)smem;
  const unsigned lbase = (unsigned)(size_t)L;
  const int tid = threadIdx.x, lane0 = tid & 63, w = __builtin_amdgcn_readfirstlane(tid >> 6);
  const int b = blockIdx.x;
  const rsrc_t rs = __builtin_amdgcn_make_buffer_rsrc((void*)ws, 0, WS_BYTES, 0x00020000);
  gu32* fb = (gu32*)(ws + OFF_FLAGS);
  const f32x4 z4 = {0, 0, 0, 0};
  const int gm = 4 * ((b & 7) >> 1) + (b >> 6), jm = 8 * (b & 1) + ((b >> 3) & 7);
  const int hh = (jm >> 2) & 1, cs = (jm & 3) | ((jm >> 3) << 2);
#define FRESH() int lane = lane0; PIN(lane); const int q = lane >> 4, n = lane & 15; const unsigned l16 = (unsigned)lane * 16u; \
  LBase lb; { unsigned o1 = l16 + 65536u, o2 = l16 + 131072u; PIN(o1); PIN(o2); lb.b[0] = L + l16; lb.b[1] = L + o1; lb.b[2] = L + o2; } (void)q; (void)n; (void)lb; \
  const unsigned char* wsp = ws; asm volatile("" : "+s"(wsp)); int jo = jm; asm volatile("" : "+s"(jo)); (void)wsp; (void)jo;
  const unsigned epoch = __hip_atomic_load(fb + FO_EPOCH / 4, RLX_AGENT) + 1u;

  v4u w2[14][4];
  auto ldw2 = [&](auto I, unsigned l16) {
    constexpr int i = I;
    w2[i >> 2][i & 3] = ldb1(rs, OFF_W2 + (unsigned)((cs * 4 + w) * 56 + i) * 1024u + l16);
  };
  {
    FRESH();
    const int g = 4 * ((b & 7) >> 1) + (b >> 6), j = 8 * (b & 1) + ((b >> 3) & 7);
    const int drow = lane >> 2, dch = (lane & 3) ^ (((drow >> 3) & 1) << 1);
    {
      const unsigned voff = (unsigned)(drow * 1536 + 32 * j + dch * 8) * 2u;
#pragma unroll
      for (int i = 0; i < 24; ++i) {
        const int it = 4 * i + w, rb = it / 3, seg = it % 3;
        dma16_in((const unsigned char*)wqkv + (size_t)(rb * 16 * 1536 + seg * 512) * 2, voff, lbase + (seg * 32 + rb) * 1024);
      }
    }
    v4u xr[4][8];
    {
      const GAS v4u* xp = (const GAS v4u*)(x + (size_t)(128 * g + 32 * w + (lane >> 5)) * 512 + (lane & 31) * 8);
#pragma unroll
      for (int blk = 0; blk < 4; ++blk)
#pragma unroll
        for (int i = 0; i < 8; ++i) xr[blk][i] = xp[((blk >> 1) * 16 + 2 * i) * 64 + (blk & 1) * 32];
    }
    const u16* psrc; int pld, pp, pks, pkind, prow[4];
    if (b < 112) { psrc = w1; pld = 1792; pp = b >> 1; pks = 8 * (b & 1) + 2 * w; pkind = 0; }
    else {
      const int u = (b - 112) * 8 + 2 * w;
      if (u < 896) { psrc = w2g; pld = 512; pp = u / 56; pks = u % 56; pkind = 1; }
      else { psrc = wo; pld = 512; pp = (u - 896) >> 4; pks = (u - 896) & 15; pkind = 2; }
    }
#pragma unroll
    for (int i = 0; i < 4; ++i) {
      const int ks = pks + (i >> 1);
      prow[i] = pkind == 1 ? 112 * (2 * (ks / 7) + (i & 1)) + 16 * (ks % 7) : 32 * ks + 16 * (i & 1);
    }
    v4u pt[4];
#pragma unroll
    for (int i = 0; i < 4; ++i) pt[i] = *(const GAS v4u*)(psrc + (size_t)(prow[i] + drow) * pld + 32 * pp + dch * 8);
    SCHED();
    if (tid < 30) stb1(rs, OFF_FLAGS + FO_CNT + b * 480 + tid * 16, v4u{0u, 0u, 0u, 0u});
    v4u xa[16], xb[16];
    float s1a = 0, s2a = 0, s1b = 0, s2b = 0;
    LAS unsigned char* B = L + L_XB + w * 8192;
    {
      const int c = lane & 31, ksl = c >> 2, kgw = c & 3, fw = (ksl & 1) * 4 + kgw;
      LAS unsigned char* wp = B + ksl * 1024 + kgw * 256;
      LAS unsigned char* rp0 = B + q * 256 + ((n ^ q) * 16);
      LAS unsigned char* rp1 = B + q * 256 + ((n ^ (4 + q)) * 16);
#pragma unroll
      for (int blk = 0; blk < 4; ++blk) {
#pragma unroll
        for (int i = 0; i < 8; ++i) *(LAS v4u*)(wp + (((2 * i + (lane >> 5)) ^ fw) * 16)) = xr[blk][i];
        LGKM0();
#pragma unroll
        for (int k8 = 0; k8 < 8; ++k8) {
          const v4u f = *(LAS v4u*)(((k8 & 1) ? rp1 : rp0) + k8 * 1024);
          if (blk < 2) xa[(blk & 1) * 8 + k8] = f; else xb[(blk & 1) * 8 + k8] = f;
#pragma unroll
          for (int e = 0; e < 4; ++e) {
            if (blk < 2) { s1a = dot2(f[e], 0x3f803f80u, s1a); s2a = dot2(f[e], f[e], s2a); }
            else { s1b = dot2(f[e], 0x3f803f80u, s1b); s2b = dot2(f[e], f[e], s2b); }
          }
        }
        PIN(s1a); PIN(s2a); PIN(s1b); PIN(s2b);
        LGKM0();
      }
    }
    const int r4 = n >> 2, c4 = n & 3;
    unsigned trb[2];
#pragma unroll
    for (int ct = 0; ct < 2; ++ct)
      trb[ct] = (unsigned)((q >> 1) * 1024 + ((q & 1) * 8 + r4) * 64 + (2 * (ct ^ (q & 1)) + (c4 >> 1)) * 16 + (c4 & 1) * 8);
    const v4u ones = {0x3f803f80u, 0x3f803f80u, 0x3f803f80u, 0x3f803f80u};
    {
#pragma unroll
      for (int i = 0; i < 4; ++i) *(LAS v4u*)(B + i * 1024 + l16) = pt[i];
      LGKM0();
      f32x4 c1[2] = {z4, z4};
#pragma unroll
      for (int u = 0; u < 2; ++u)
#pragma unroll
        for (int ct = 0; ct < 2; ++ct) {
          const v4u f = tr_frag(B + u * 2048 + trb[ct]);
          const int gct = 2 * pp + ct, ks = pks + u;
          unsigned dst;
          if (pkind == 0) dst = OFF_W1 + (unsigned)((gct / 7) * 112 + (gct % 7) * 16 + ks) * 1024u;
          else if (pkind == 1) { const int p = ks / 7, t = ks % 7; dst = OFF_W2 + (unsigned)(((gct >> 2) * 4 + (p >> 1)) * 56 + (2 * t + (p & 1)) * 4 + (gct & 3)) * 1024u; }
          else dst = OFF_WO + (unsigned)((gct >> 2) * 64 + (gct & 3) * 16 + ks) * 1024u;
          stb1(rs, dst + l16, f);
          c1[ct] = mfma(f, ones, c1[ct]);
        }
      if (pkind == 0 && n == 0) {
        const int part = (b & 1) * 4 + w;
#pragma unroll
        for (int ct = 0; ct < 2; ++ct)
          stb1(rs, OFF_CS1 + (unsigned)(part * 1792 + 32 * pp + 16 * ct + 4 * q) * 4u, __builtin_bit_cast(v4u, c1[ct]));
      }
    }
    Stats st;
    finish_stats(sum_q(s1a), sum_q(s2a), st.a[0], st.b[0]);
    finish_stats(sum_q(s1b), sum_q(s2b), st.a[1], st.b[1]);
    if (q == 0) {
      *(LAS f32x2*)(L + L_STX + w * 256 + n * 8) = f32x2{st.a[0], st.b[0]};
      *(LAS f32x2*)(L + L_STX + w * 256 + 128 + n * 8) = f32x2{st.a[1], st.b[1]};
    }
    DRAIN();
    BAR();
    {
      f32x4 cs[6] = {z4, z4, z4, z4, z4, z4};
#pragma unroll
      for (int i = 0; i < 4; ++i)
#pragma unroll
        for (int seg = 0; seg < 3; ++seg) {
          LAS unsigned char* blk = L + seg * 32768 + (4 * w + i) * 2048;
          const v4u f0 = tr_frag(blk + trb[0]), f1 = tr_frag(blk + trb[1]);
          const int t0 = seg == 0 ? 4 : seg - 1, t1 = seg == 0 ? 5 : seg + 1;
          if (seg == 0) { cs[4] = mfma(f0, ones, cs[4]); cs[5] = mfma(f1, ones, cs[5]); }
          else { cs[seg - 1] = mfma(ones, f0, cs[seg - 1]); cs[seg + 1] = mfma(ones, f1, cs[seg + 1]); }
          *(LAS v4u*)(blk + l16) = f0;
          *(LAS v4u*)(blk + 1024 + l16) = f1;
          if (4 * w + i == g) {
            stb1(rs, OFF_WQKV + (unsigned)(j * 96 + t0 * 16 + g) * 1024u + l16, f0);
            stb1(rs, OFF_WQKV + (unsigned)(j * 96 + t1 * 16 + g) * 1024u + l16, f1);
          }
        }
      LAS float* csw = (LAS float*)(L + L_CSP) + w * 96;
#pragma unroll
      for (int t = 4; t < 6; ++t) if (n == 0) *(LAS f32x4*)(csw + t * 16 + 4 * q) = cs[t];
#pragma unroll
      for (int t = 0; t < 4; ++t) if (q == 0) csw[t * 16 + n] = cs[t][0];
    }
    BAR();
    if (tid < 96) {
      LAS float* c = (LAS float*)(L + L_CSP);
      const float s = (c[tid] + c[96 + tid]) + (c[192 + tid] + c[288 + tid]);
      ((LAS float*)(L + L_CSQ))[tid] = s;
      if (g == 0) __hip_atomic_store((GAS float*)(ws + OFF_CSQ) + j * 96 + tid, s, RLX_AGENT);
    }
    DRAIN();
    BAR();
    if (tid == 0) __hip_atomic_store(fb + FO_PRE / 4 + b, epoch, RLX_AGENT);
    f32x4 acc[6][2];
    {
      constexpr int FD = 6, NF = 96;
      auto addr = [&](int i) -> unsigned {
        const int t = i % 6, ks = i / 6;
        const int seg = t < 4 ? 1 + (t & 1) : 0, ct = t < 4 ? (t >> 1) : (t - 4);
        return (unsigned)(seg * 32768 + ks * 2048 + ct * 1024);
      };
      v4u wf[FD];
#pragma unroll
      for (int i = 0; i < FD; ++i) wf[i] = ldsf(lb, addr(i));
      SCHED();
      static_for<NF>([&](auto I) {
        constexpr int i = I, t = i % 6, ks = i / 6;
        const v4u f = wf[i % FD];
        if constexpr (i + FD < NF) wf[i % FD] = ldsf(lb, addr(i + FD));
        if constexpr (t < 4) mfma2<ks == 0>(acc[t][0], acc[t][1], xa[ks], f, xb[ks], f);
        else mfma2<ks == 0>(acc[t][0], acc[t][1], f, xa[ks], f, xb[ks]);
        SCHED();
      });
    }
    MFMA_FENCE();
#pragma unroll
    for (int t = 0; t < 6; ++t) { PINV(acc[t][0]); PINV(acc[t][1]); }
    f32x4 kva, kvb, za, zb;
    kv_epilogue(acc, L + L_STX + w * 256, (LAS float*)(L + L_CSQ), lane, kva, kvb, za, zb);
    v4u pq0[2];
    q_epilogue(acc, st, (LAS float*)(L + L_CSQ), lane, pq0);
    const int cu = g * 16 + j;
#pragma unroll
    for (int rt = 0; rt < 2; ++rt) stb1(rs, OFF_PQ + (unsigned)((cu * 4 + w) * 2 + rt) * 1024u + l16, pq0[rt]);
    if (w == 0) {
      for (unsigned sp = 0;;) {
        const v4u v = ldb1(rs, OFF_FLAGS + FO_PRE + l16);
        if (__all(v.x == epoch && v.y == epoch && v.z == epoch && v.w == epoch)) break;
        __builtin_amdgcn_s_sleep(4);
        if (spin_fail(sp, fb, epoch)) break;
      }
    }
    kv_publish(L, L_KVX, rs, OFF_KVP + (unsigned)(j * 16 + g) * KVREC, lane, w, kva, kvb, za, zb);
    publish(fb, FC_KV + 2 * j, tid);
  }

  const int g = gm, j = jm;
  const int cu = g * 16 + j;
  const int myrt = 8 * g + 4 * hh + w;
  const int rt0 = 8 * g + 2 * w;
  v4u pq[2];
  v2u res[4];
  dma_w123<64>(ws, OFF_WO + cs * 65536u, lbase + WOU[0] * LU, w, lane0);
  dma_w123<32>(ws, OFF_W1 + j * 114688u, lbase + W1U[0][0] * LU, w, lane0);
  static_for<14>([&](auto I) { ldw2(I, (unsigned)lane0 * 16u); });
  {
    FRESH();
    const GAS v2u* xp = (const GAS v2u*)(x + (size_t)(16 * myrt + n) * 512 + 64 * cs + 4 * q);
#pragma unroll
    for (int c = 0; c < 4; ++c) res[c] = xp[c * 4];
    if (tid < 112) {
      float csv = 0.f;
#pragma unroll
      for (int i = 0; i < 8; ++i) csv += __hip_atomic_load((GAS float*)(ws + OFF_CS1) + i * 1792 + 112 * j + tid, RLX_AGENT);
      ((LAS float*)(L + L_CS1))[tid] = csv;
    } else if (tid >= 128 && tid < 224) {
      ((LAS float*)(L + L_CSQ))[tid - 128] = __hip_atomic_load((GAS float*)(ws + OFF_CSQ) + j * 96 + tid - 128, RLX_AGENT);
    }
  }
  using std::integral_constant;
#define IC(x) integral_constant<int, (x)>{}
#define VMW(n) asm volatile("s_waitcnt vmcnt(%0)" :: "i"(n) : "memory")

  static_for<2>([&](auto LL) {
    constexpr int l = LL;
    if (w == 0) poll_ge(fb, FC_KV + (l * 16 + j) * 2, 16u, epoch);
    BAR();
    {
      FRESH();
      if constexpr (l == 0) {
#pragma unroll
        for (int rt = 0; rt < 2; ++rt) pq[rt] = ldb1(rs, OFF_PQ + (unsigned)((cu * 4 + w) * 2 + rt) * 1024u + l16);
      }
      const unsigned rb = OFF_KVP + (unsigned)((l * 16 + j) * 16) * KVREC;
      if (w < 3) {
        const unsigned o = rb + (w < 2 ? (unsigned)w * 1024u + l16 : 2048u + (unsigned)(lane & 7) * 16u);
        v4u a[16];
#pragma unroll
        for (int i = 0; i < 16; ++i) a[i] = ldb1(rs, o + i * KVREC);
        SCHED();
        f32x4 s[4];
#pragma unroll
        for (int i = 0; i < 4; ++i)
          s[i] = (__builtin_bit_cast(f32x4, a[4 * i]) + __builtin_bit_cast(f32x4, a[4 * i + 1])) + (__builtin_bit_cast(f32x4, a[4 * i + 2]) + __builtin_bit_cast(f32x4, a[4 * i + 3]));
        const f32x4 t = (s[0] + s[1]) + (s[2] + s[3]);
        const v2u pk = v2u{pack2(t[0], t[1]), pack2(t[2], t[3])};
        if (w < 2) *(LAS v2u*)(L + L_KVX + w * 512 + lane * 8) = pk;
        else if (lane < 8) *(LAS v2u*)(L + L_KVX + 1024 + lane * 8) = pk;
      }
      BAR();
      const v2u ka = *(LAS v2u*)(L + L_KVX + lane * 8), kb = *(LAS v2u*)(L + L_KVX + 512 + lane * 8);
      const v2u zz = *(LAS v2u*)(L + L_KVX + 1024 + ((n & 1) * 4 + q) * 8);
      const v4u Aa = {ka.x, ka.y, 0u, 0u};
      const v4u Ab = {0u, 0u, kb.x, kb.y};
      const bool za_ = (n & 3) == 0, zb_ = (n & 3) == 1;
      const v4u Az = {za_ ? zz.x : 0u, za_ ? zz.y : 0u, zb_ ? zz.x : 0u, zb_ ? zz.y : 0u};
#pragma unroll
      for (int rt = 0; rt < 2; ++rt) {
        const f32x4 na = mfma(Aa, pq[rt], z4), nb = mfma(Ab, pq[rt], z4), dn = mfma(Az, pq[rt], z4);
        const float ra = __builtin_amdgcn_rcpf(dn[0] + 1e-6f), rb2 = __builtin_amdgcn_rcpf(dn[1] + 1e-6f);
        const v4u o = pair16(pack2(na[0] * ra, na[1] * ra), pack2(na[2] * ra, na[3] * ra), pack2(nb[0] * rb2, nb[1] * rb2), pack2(nb[2] * rb2, nb[3] * rb2));
        const unsigned kg = (q & 1) * 2 + (q >> 1);
        stb1(rs, OFF_O + l * XF_BYTES + (unsigned)((rt0 + rt) * 16 + j) * 1024u + (kg * 16 + n) * 16, o);
      }
      DRAIN();
      arrive(fb, FC_O + (l * 16 + g) * 4 + w, 0, lane);
      BAR();
      if constexpr (l == 0) {
        static_for<12>([&](auto K) {
          const unsigned pc = (unsigned)((K & 3) * 4 + w) * 1024u;
          dma16(wsp + OFF_W1 + jo * 114688u + (2 + (K >> 2)) * LU + pc, l16, lbase + W1U[0][2 + (K >> 2)] * LU + pc);
        });
      } else {
        static_for<8>([&](auto K) {
          const unsigned pc = (unsigned)((K & 3) * 4 + w) * 1024u;
          dma16(wsp + OFF_W1 + jo * 114688u + (3 + (K >> 2)) * LU + pc, l16, lbase + W1U[1][3 + (K >> 2)] * LU + pc);
        });
      }
    }
    poll_ge(fb, FC_O + (l * 16 + g) * 4 + ((4 * hh + w) >> 1), 16u, epoch);
    {
      FRESH();
      v4u a[16];
      const unsigned ab = OFF_O + l * XF_BYTES + (unsigned)(myrt * 16) * 1024u + l16;
      constexpr int AD = 8;
#pragma unroll
      for (int ks = 0; ks < AD; ++ks) a[ks] = ldb1(rs, ab + ks * 1024);
      f32x4 acc[4];
      constexpr int FD = 8;
      v4u wf[FD];
#pragma unroll
      for (int i = 0; i < FD; ++i) wf[i] = ldsf(lb, (WOU[l] + (i & 3)) * LU + (i >> 2) * 1024);
      SCHED();
      static_for<64>([&](auto I) {
        constexpr int i = I;
        const v4u f = wf[i % FD];
        if constexpr (i + FD < 64) wf[i % FD] = ldsf(lb, (WOU[l] + ((i + FD) & 3)) * LU + ((i + FD) >> 2) * 1024);
        mfma1<(i < 4)>(acc[i & 3], f, a[i >> 2]);
        if constexpr ((i & 3) == 1 && (i >> 2) + AD < 16) a[(i >> 2) + AD] = ldb1(rs, ab + ((i >> 2) + AD) * 1024);
        SCHED();
      });
      MFMA_FENCE();
      PINV(acc[0]); PINV(acc[1]); PINV(acc[2]); PINV(acc[3]);
      res_update(res, acc);
      store_x_tile(rs, OFF_X1 + l * XF_BYTES, OFF_ST + l * ST_BYTES, res, myrt, cs, lane);
      DRAIN();
      arrive(fb, FC_X1 + (l * 16 + g) * 8 + 4 * hh + w, 0, lane);
      BAR();
      static_for<8>([&](auto K) {
        const unsigned pc = (unsigned)((K & 3) * 4 + w) * 1024u;
        dma16(wsp + OFF_W1 + jo * 114688u + (5 + (K >> 2)) * LU + pc, l16, lbase + W1U[l][5 + (K >> 2)] * LU + pc);
      });
      if constexpr (l == 0) static_for<14>([&](auto I) { ldw2(IC(14 + I), l16); });
    }

    poll_ge(fb, FC_X1 + (l * 16 + g) * 8 + 2 * w, 8u, epoch);
    poll_ge(fb, FC_X1 + (l * 16 + g) * 8 + 2 * w + 1, 8u, epoch);
    {
      FRESH();
      const int hline_out = FC_H + ((l * 16 + g) * 2 + (w >> 1)) * 4 + (j >> 2), hline_in = FC_H + ((l * 16 + g) * 2 + hh) * 4 + w;
      constexpr int WIN = 16;
      v4u a[WIN];
      unsigned hb[4];
#pragma unroll
      for (int i4 = 0; i4 < 4; ++i4) hb[i4] = OFF_H + l * HF_BYTES + (unsigned)((8 * g + 4 * hh + ((w + i4) & 3)) * 56 + 14 * w) * 1024u + l16;
      auto hoff = [](int f) -> unsigned { return (unsigned)(((f >> 2) & 1) * 7 + (f >> 3)) * 1024u; };
      {
        const unsigned ab = OFF_X1 + l * XF_BYTES + (unsigned)(rt0 * 16) * 1024u + l16;
        v4u xa[16], xb[16], sp[2];
        constexpr int AD = 6, FD = 6, NF = 112;
        load_stats(rs, OFF_ST + l * ST_BYTES, rt0, lane, sp);
#pragma unroll
        for (int ks = 0; ks < AD; ++ks) { xa[ks] = ldb1(rs, ab + ks * 1024); xb[ks] = ldb1(rs, ab + (16 + ks) * 1024); }
        VMW(2 * AD + 2);
        BAR();
        constexpr auto ft = [](int i) { return i < 64 ? (i & 3) : 4 + (i - 64) % 3; };
        constexpr auto fk = [](int i) { return i < 64 ? (i >> 2) : (i - 64) / 3; };
        auto faddr = [&](int i) -> unsigned { return (unsigned)(W1U[l][ft(i)] * LU + fk(i) * 1024); };
        f32x4 acc[7][2];
        v4u wf[FD];
#pragma unroll
        for (int i = 0; i < FD; ++i) wf[i] = ldsf(lb, faddr(i));
        SCHED();
        float sa[2], sb[2];
        const unsigned hst = OFF_H + l * HF_BYTES + ((unsigned)(rt0 + (q & 1)) * 56u + (unsigned)(j >> 1) * 7u) * 1024u + ((2 * (j & 1) + (q >> 1)) * 16 + n) * 16;
        auto epi = [&](auto T) {
          constexpr int t = T;
          const f32x4 c = *(LAS f32x4*)(L + L_CS1 + (t * 16 + 4 * q) * 4);
          float y[8];
#pragma unroll
          for (int i = 0; i < 8; ++i) {
            const float v = fmaf(acc[t][i >> 2][i & 3], sa[i >> 2], sb[i >> 2] * c[i & 3]);
            y[i] = v * __builtin_amdgcn_rcpf(fmaf(__builtin_amdgcn_exp2f(-v), LOG2E, LOG2E));
          }
          stb1(rs, hst + t * 1024, pair16(pack2(y[0], y[1]), pack2(y[2], y[3]), pack2(y[4], y[5]), pack2(y[6], y[7])));
        };
        auto kloop = [&](auto LO, auto N) {
          static_for<N>([&](auto I) {
            constexpr int i = LO + I, t = ft(i), ks = fk(i);
            const v4u f = wf[i % FD];
            if constexpr (i + FD < NF) wf[i % FD] = ldsf(lb, faddr(i + FD));
            mfma2<ks == 0>(acc[t][0], acc[t][1], f, xa[ks], f, xb[ks]);
            if constexpr (i < 64 && t == 1 && ks + AD < 16) { xa[ks + AD] = ldb1(rs, ab + (ks + AD) * 1024); xb[ks + AD] = ldb1(rs, ab + (16 + ks + AD) * 1024); }
            if constexpr (i == 94) { VMW(0); arrive(fb, hline_out, 0, lane); }
            if constexpr (l == 0 && i >= 96 && i < 110) { ldw2(IC(28 + 2 * (i - 96)), l16); ldw2(IC(29 + 2 * (i - 96)), l16); }
            SCHED();
          });
          MFMA_FENCE();
          static_for<ft(LO + N - 1) - ft(LO) + 1>([&](auto T) { PINV(acc[ft(LO) + T][0]); PINV(acc[ft(LO) + T][1]); });
        };
        kloop(IC(0), IC(64));
        {
          Stats st;
          make_stats(sp, st);
          sa[0] = st.a[0] * LOG2E; sa[1] = st.a[1] * LOG2E; sb[0] = st.b[0] * LOG2E; sb[1] = st.b[1] * LOG2E;
        }
        static_for<4>([&](auto T) { epi(T); });
        SCHED();
        kloop(IC(64), IC(48));
        poll_dw(fb, hline_in, 0, 8u, epoch);
#pragma unroll
        for (int f = 0; f < WIN; ++f) a[f] = ldb1(rs, hb[f & 3] + hoff(f));
        static_for<3>([&](auto T) { epi(IC(4 + T)); });
      }
      BAR();
      f32x4 acc2[4][4];
      static_for<56>([&](auto F) {
        constexpr int f = F, wi = 2 * (f >> 3) + ((f >> 2) & 1);
        const v4u cur = a[f % WIN];
        if constexpr (f == 10) { VMW(10); arrive(fb, hline_out, 1, lane); }
        if constexpr (f + WIN < 56) {
          if constexpr (f + WIN == 32) poll_dw(fb, hline_in, 1, 8u, epoch);
          a[f % WIN] = ldb1(rs, hb[(f + WIN) & 3] + hoff(f + WIN));
        }
        mfma4a<(f < 4)>(acc2[f & 3], w2[wi][0], w2[wi][1], w2[wi][2], w2[wi][3], cur);
        if constexpr (l == 0 && f >= 20 && f < 44) {
          const unsigned o = (unsigned)((f - 20) * 4 + w) * 1024u;
          dma16(wsp + OFF_WQKV + jo * 98304u + o, l16, lbase + QU * LU + o);
        }
        SCHED();
      });
      MFMA_FENCE();
#pragma unroll
      for (int i4 = 0; i4 < 4; ++i4)
#pragma unroll
        for (int c = 0; c < 4; ++c) PINV(acc2[i4][c]);
#pragma unroll
      for (int i4 = 1; i4 < 4; ++i4) {
        const unsigned d = (w + i4) & 3;
#pragma unroll
        for (int c = 0; c < 4; ++c) *(LAS f32x4*)(L + REDU[l][i4 - 1] * LU + (d * 4 + c) * 1024 + l16) = acc2[i4][c];
      }
      BAR();
#pragma unroll
      for (int sl = 0; sl < 3; ++sl)
#pragma unroll
        for (int c = 0; c < 4; ++c) acc2[0][c] += *(LAS f32x4*)(L + REDU[l][sl] * LU + (w * 4 + c) * 1024 + l16);
      res_update(res, acc2[0]);
      if constexpr (l == 0) {
        store_x_tile(rs, OFF_X2, OFF_ST + 2 * ST_BYTES, res, myrt, cs, lane);
      } else {
#pragma unroll
        for (int c2 = 0; c2 < 2; ++c2) {
          const v4u o = pair16(res[2 * c2].x, res[2 * c2].y, res[2 * c2 + 1].x, res[2 * c2 + 1].y);
          *(GAS v4u*)(out + (size_t)(16 * myrt + n) * 512 + 64 * cs + 16 * (2 * c2 + (q & 1)) + 8 * (q >> 1)) = o;
        }
      }
      if constexpr (l == 0) {
        DRAIN();
        arrive(fb, FC_X2 + g * 8 + 4 * hh + w, 0, lane);
      }
    }
    if constexpr (l == 0) {
      poll_ge(fb, FC_X2 + g * 8 + 2 * w, 8u, epoch);
      poll_ge(fb, FC_X2 + g * 8 + 2 * w + 1, 8u, epoch);
      FRESH();
      const unsigned ab = OFF_X2 + (unsigned)(rt0 * 16) * 1024u + l16;
      v4u sp[2];
      load_stats(rs, OFF_ST + 2 * ST_BYTES, rt0, lane, sp);
      BAR();
      f32x4 acc[6][2];
      Stats st;
      gemm_k<6, 4, 6, 6>(rs, ab, lb, acc, [](int t, int ks) -> unsigned { return (unsigned)((QU + t) * LU + ks * 1024); },
        [&](auto I) {
          constexpr int i = I;
          if constexpr (i == 72) {
            make_stats(sp, st);
            if (q == 0) {
              *(LAS f32x2*)(L + L_STX + w * 256 + n * 8) = f32x2{st.a[0], st.b[0]};
              *(LAS f32x2*)(L + L_STX + w * 256 + 128 + n * 8) = f32x2{st.a[1], st.b[1]};
            }
          }
          if constexpr ((i & 7) == 4) {
            constexpr int k = i >> 3;
            const unsigned pc = (unsigned)((k & 3) * 4 + w) * 1024u;
            dma16(wsp + OFF_W1 + jo * 114688u + (k >> 2) * LU + pc, l16, lbase + W1U[1][k >> 2] * LU + pc);
          }
        });
      f32x4 kva, kvb, za, zb;
      kv_epilogue(acc, L + L_STX + w * 256, (LAS float*)(L + L_CSQ), lane, kva, kvb, za, zb);
      kv_publish(L, L_KVX, rs, OFF_KVP + (unsigned)((16 + j) * 16 + g) * KVREC, lane, w, kva, kvb, za, zb);
      publish(fb, FC_KV + (16 + j) * 2, tid);
      dma_w123<64>(ws, OFF_WO + cs * 65536u, lbase + WOU[1] * LU, w, lane0);
      q_epilogue(acc, st, (LAS float*)(L + L_CSQ), lane, pq);
      if (b == 0 && tid == 0) __hip_atomic_store(fb + FO_EPOCH / 4, epoch, RLX_AGENT);
    }
  });
}

}

extern "C" void kernel_launch(void* const* d_in, const int* in_sizes, int n_in, void* d_out, int out_size, void* d_ws, size_t ws_size,
                              hipStream_t stream) {
  const u16* x = (const u16*)d_in[0];
  const u16* wqkv = (const u16*)d_in[1];
  const u16* wo = (const u16*)d_in[2];
  const u16* w1 = (const u16*)d_in[4];
  const u16* w2 = (const u16*)d_in[5];
  (void)hipFuncSetAttribute((const void*)kmain, hipFuncAttributeMaxDynamicSharedMemorySize, K2_LDS);
  kmain<<<256, 256, K2_LDS, stream>>>(x, wqkv, wo, w1, w2, (u16*)d_out, (unsigned char*)d_ws);
}
```
